```python
import torch

def _softmax(x, axis=-1):
    return torch.softmax(x, dim=axis)
def _gelu(x):
    return torch.nn.functional.gelu(x, approximate="tanh")
def _silu(x):
    return torch.nn.functional.silu(x)
def _sigmoid(x):
    return torch.sigmoid(x)
def _layernorm(x, axis=-1, eps=1e-5):
    m = x.mean(dim=axis, keepdim=True)
    v = x.var(dim=axis, keepdim=True, unbiased=False)
    return (x - m) / torch.sqrt(v + eps)
def _scatter_add(tgt, idx, src, axis=0):
    return tgt.clone().index_add_(axis, idx, src)
def _segment_sum(src, seg, n):
    out = torch.zeros((n,) + src.shape[1:], dtype=src.dtype)
    return out.index_add_(0, seg, src)
def _pad(x, pw):
    flat = tuple(v for pair in reversed(pw) for v in pair)
    return torch.nn.functional.pad(x, flat)

kernel_name = 'sequential_preLN__mha_block__moe_arch_mod__prompt'

def setup_inputs():

    _w = (lambda a: (lambda t: t.to(torch.bfloat16) if t.is_floating_point() else t)(torch.as_tensor(a)))
    def _A_setup():
        B, S, D, H = 1, 2048, 576, 32
        return {
            "x": _w((lambda *s: torch.randn(*s, dtype=torch.bfloat16) * 0.05)(B, S, D)),
            "wqkv": _w((lambda *s: torch.randn(*s, dtype=torch.bfloat16) * 0.05)(D, 3 * D)),
            "wo": _w((lambda *s: torch.randn(*s, dtype=torch.bfloat16) * 0.05)(D, D)),
        }
    _A_in = _A_setup()

    def _B_setup():
        B, S, D, F, C = 1, 2048, 576, 4608, 1
        return {
            "x": _w((lambda *s: torch.randn(*s, dtype=torch.bfloat16) * 0.05)(B, S, D)),
            "w_r": _w((lambda *s: torch.randn(*s, dtype=torch.bfloat16) * 0.05)(D)),
            "w_gate": _w((lambda *s: torch.randn(*s, dtype=torch.bfloat16) * 0.05)(D, F)),
            "w_up": _w((lambda *s: torch.randn(*s, dtype=torch.bfloat16) * 0.05)(D, F)),
            "w_down": _w((lambda *s: torch.randn(*s, dtype=torch.bfloat16) * 0.05)(F, D)),
        }
    _B_in = _B_setup()

    return {
        "A_x": _A_in["x"],
        "A_wqkv": _A_in["wqkv"],
        "A_wo": _A_in["wo"],
        "B_x": _B_in["x"],
        "B_w_r": _B_in["w_r"],
        "B_w_gate": _B_in["w_gate"],
        "B_w_up": _B_in["w_up"],
        "B_w_down": _B_in["w_down"],
    }

def reference(A_x, A_wqkv, A_wo, B_x, B_w_r, B_w_gate, B_w_up, B_w_down):
    def _A_ref(x, wqkv, wo):
        B, S, D = x.shape
        H = 32
        Dh = D // H
        qkv = (lambda x, *d: x.permute(*d))((x @ wqkv).reshape(B, S, 3, H, Dh), 2, 0, 3, 1, 4)
        q, k, v = qkv[0], qkv[1], qkv[2]
        scores = torch.einsum("bhqd,bhkd->bhqk", q, k) * (Dh ** -0.5)
        p = _softmax(scores, axis=-1)
        o = torch.einsum("bhqk,bhkd->bhqd", p, v)
        return (lambda x, *d: x.permute(*d))(o, 0, 2, 1, 3).reshape(B, S, D) @ wo

    def _B_ref(x, w_r, w_gate, w_up, w_down):
        B, S, D = x.shape
        C = 1
        r = x @ w_r
        idx = torch.argsort(-r, axis=-1)[:, :C]
        idx_e = (lambda x, n, axis: torch.repeat_interleave(x, n, dim=axis))(idx.reshape(B, C, 1), D, axis=2)
        x_sel = (lambda x, i, axis: torch.take_along_dim(x, i, dim=axis))(x, idx_e, axis=1)
        g = _silu(x_sel @ w_gate)
        u = x_sel @ w_up
        y = (g * u) @ w_down
        r_sel = _sigmoid((lambda x, i, axis: torch.take_along_dim(x, i, dim=axis))(r, idx, axis=1))
        y = y * r_sel.reshape(B, C, 1)
        b_off = (torch.arange(B) * S).reshape(B, 1)
        flat_idx = (idx + b_off).reshape(B * C)
        zero = (x * 0.0).reshape(B * S, D)
        upd = _scatter_add(zero, flat_idx, y.reshape(B * C, D), axis=0)
        return x + upd.reshape(B, S, D)

    x = A_x
    h = x + _A_ref(_layernorm(x, axis=-1), A_wqkv, A_wo)
    h2 = h + _B_ref(_layernorm(h, axis=-1), B_w_r, B_w_gate, B_w_up, B_w_down)
    return h2
```

```cpp
#include <hip/hip_runtime.h>
#include <hip/hip_bf16.h>
#include <stdint.h>

typedef __attribute__((ext_vector_type(4))) float f32x4;
typedef __attribute__((ext_vector_type(16))) float f32x16;
typedef __attribute__((ext_vector_type(32))) float f32x32;
typedef __attribute__((ext_vector_type(8))) __bf16 bf16x8;
typedef __attribute__((ext_vector_type(2))) __bf16 bf16x2;
typedef __attribute__((ext_vector_type(4))) short s16x4;
typedef __attribute__((ext_vector_type(8))) short s16x8;
typedef __attribute__((ext_vector_type(4))) unsigned u32x4;
typedef __attribute__((ext_vector_type(2))) unsigned u32x2;
typedef __attribute__((address_space(3))) s16x4* lds_s16x4_ptr;

#define DI __device__ __forceinline__

constexpr int S_ = 2048, D_ = 576, F_ = 4608;
constexpr float LN_EPS = 1e-5f;

constexpr int QCH = 9216;
constexpr int KCH = 18944;
constexpr int QIMG = 8 * QCH;
constexpr int KVIMG = 155648;
constexpr size_t WS_Q = 0;
constexpr size_t WS_KV = WS_Q + (size_t)32 * QIMG;
constexpr size_t WS_O = WS_KV + (size_t)32 * KVIMG;
constexpr size_t WS_WO = WS_O + (size_t)128 * 18 * 1024;
constexpr size_t WS_H = WS_WO + (size_t)36 * 18 * 1024;
constexpr size_t WS_ST = WS_H + (size_t)S_ * D_ * 2;
constexpr size_t WS_YP = WS_ST + (size_t)12 * S_ * 4;
constexpr size_t WS_SEL = WS_YP + (size_t)144 * D_ * 4;

DI float bf2f(unsigned short v) { return __builtin_bit_cast(float, (unsigned)v << 16); }
DI float bflo(unsigned v) { return __builtin_bit_cast(float, v << 16); }
DI float bfhi(unsigned v) { return __builtin_bit_cast(float, v & 0xffff0000u); }
DI unsigned pk2(float a, float b) {
    bf16x2 r; r[0] = (__bf16)a; r[1] = (__bf16)b;
    return __builtin_bit_cast(unsigned, r);
}
DI float rbf(float a) { return (float)(__bf16)a; }

DI void glds16(const void* g, void* lds) {
    __builtin_amdgcn_global_load_lds((const __attribute__((address_space(1))) unsigned*)g,
                                     (__attribute__((address_space(3))) unsigned*)lds, 16, 0, 0);
}

#define XLOAD(dst, ptr, off) asm volatile("global_load_dwordx4 %0, %1, off offset:" #off : "=v"(dst) : "v"(ptr) : "memory")

template <bool SWAP>
DI void k1_body(char* smem, const char* __restrict__ x, const char* __restrict__ wqkv, char* __restrict__ ws,
                int mt, int j, int w, int l) {
    const int g = l >> 4, i16 = l & 15;
    bf16x8 xa[2][18];
    {
        const int up = i16 >> 1, hf = i16 & 1;
        const int f = g | ((w >> 1) << 2);
        int u = up ^ f; u = u > 6 ? 6 : u;
        const char* wsrc = wqkv + (size_t)(4 * w + g) * 3456 + ((216 * j) & ~15) + u * 32 + hf * 16;
        const char* xs0 = x + (size_t)(mt * 128 + 32 * w + i16) * 1152 + g * 16;
        const char* xs1 = xs0 + 16 * 1152;
        char* ldst = smem + w * 1024;
#define K1_ISSUE(ks) \
        glds16(wsrc + (size_t)(2 * ks) * 55296, ldst + (8 * ks) * 1024); \
        glds16(wsrc + (size_t)(2 * ks + 1) * 55296, ldst + (8 * ks + 4) * 1024); \
        asm volatile("global_load_dwordx4 %0, %1, off offset:%2" : "=v"(xa[0][ks]) : "v"(xs0), "i"(64 * ks) : "memory"); \
        asm volatile("global_load_dwordx4 %0, %1, off offset:%2" : "=v"(xa[1][ks]) : "v"(xs1), "i"(64 * ks) : "memory");
        K1_ISSUE(0) K1_ISSUE(1) K1_ISSUE(2) K1_ISSUE(3) K1_ISSUE(4) K1_ISSUE(5)
        K1_ISSUE(6) K1_ISSUE(7) K1_ISSUE(8) K1_ISSUE(9) K1_ISSUE(10) K1_ISSUE(11)
        K1_ISSUE(12) K1_ISSUE(13) K1_ISSUE(14) K1_ISSUE(15) K1_ISSUE(16) K1_ISSUE(17)
#undef K1_ISSUE
    }
    f32x4 acc[2][7];
    f32x4 csacc[2];
#pragma unroll
    for (int n = 0; n < 7; ++n) { acc[0][n] = (f32x4)0.f; acc[1][n] = (f32x4)0.f; }
    csacc[0] = (f32x4)0.f; csacc[1] = (f32x4)0.f;
    float sx[2] = {0.f, 0.f}, sxx[2] = {0.f, 0.f};
    bf16x8 ones;
#pragma unroll
    for (int e = 0; e < 8; ++e) ones[e] = (__bf16)1.0f;
    bf16x2 one2; one2[0] = (__bf16)1.0f; one2[1] = (__bf16)1.0f;

    const int q = i16 >> 2, p = i16 & 3;
    const int fl = q | ((g & 1) << 2);
    const unsigned rbase = (unsigned)(uintptr_t)(smem) + (8 * g + q) * 256 + p * 8;
    unsigned ra[7];
#pragma unroll
    for (int n = 0; n < 7; ++n) ra[n] = rbase + ((n ^ fl) << 5);
    const unsigned rc0 = rbase + (((2 * w) ^ fl) << 5), rc1 = rbase + (((2 * w + 1) ^ fl) << 5);

    auto wfrag = [&](unsigned a, int ks) -> bf16x8 {
        s16x4 lo = __builtin_amdgcn_ds_read_tr16_b64_v4i16((lds_s16x4_ptr)(a + ks * 8192));
        s16x4 hi = __builtin_amdgcn_ds_read_tr16_b64_v4i16((lds_s16x4_ptr)(a + ks * 8192 + 1024));
        return __builtin_bit_cast(bf16x8, __builtin_shufflevector(lo, hi, 0, 1, 2, 3, 4, 5, 6, 7));
    };
    bf16x8 wfb[2][9];
    auto dots = [&](int ks, int idx) {
        const int mi = idx >> 2;
        u32x4 xv = __builtin_bit_cast(u32x4, xa[mi][ks]);
        const unsigned t = xv[idx & 3];
        bf16x2 v = __builtin_bit_cast(bf16x2, t);
        sx[mi] = __builtin_amdgcn_fdot2_f32_bf16(v, one2, sx[mi], false);
        sxx[mi] = __builtin_amdgcn_fdot2_f32_bf16(v, v, sxx[mi], false);
        asm volatile("" : "+v"(sx[mi]), "+v"(sxx[mi]));
    };
    auto kstep = [&](int ks) {
        const int cu = ks & 1, nx = cu ^ 1;
#pragma unroll
        for (int n = 0; n < 7; ++n) {
            if (ks < 17) wfb[nx][n] = wfrag(ra[n], ks + 1);
#pragma unroll
            for (int mi = 0; mi < 2; ++mi)
                acc[mi][n] = SWAP ? __builtin_amdgcn_mfma_f32_16x16x32_bf16(wfb[cu][n], xa[mi][ks], acc[mi][n], 0, 0, 0)
                                  : __builtin_amdgcn_mfma_f32_16x16x32_bf16(xa[mi][ks], wfb[cu][n], acc[mi][n], 0, 0, 0);
            dots(ks, n);
            __builtin_amdgcn_sched_barrier(0);
        }
        if (ks < 17) { wfb[nx][7] = wfrag(rc0, ks + 1); wfb[nx][8] = wfrag(rc1, ks + 1); }
        csacc[0] = SWAP ? __builtin_amdgcn_mfma_f32_16x16x32_bf16(wfb[cu][7], ones, csacc[0], 0, 0, 0)
                        : __builtin_amdgcn_mfma_f32_16x16x32_bf16(ones, wfb[cu][7], csacc[0], 0, 0, 0);
        csacc[1] = SWAP ? __builtin_amdgcn_mfma_f32_16x16x32_bf16(wfb[cu][8], ones, csacc[1], 0, 0, 0)
                        : __builtin_amdgcn_mfma_f32_16x16x32_bf16(ones, wfb[cu][8], csacc[1], 0, 0, 0);
        dots(ks, 7);
        __builtin_amdgcn_sched_barrier(0);
    };
#define K1_WAIT(k0, N) \
    asm volatile("s_waitcnt vmcnt(" #N ")" : "+v"(xa[0][k0]), "+v"(xa[1][k0]), "+v"(xa[0][k0 + 1]), "+v"(xa[1][k0 + 1]), \
                 "+v"(xa[0][k0 + 2]), "+v"(xa[1][k0 + 2]) :: "memory"); \
    __builtin_amdgcn_s_barrier(); \
    __builtin_amdgcn_sched_barrier(0);
    K1_WAIT(0, 60)
#pragma unroll
    for (int n = 0; n < 7; ++n) wfb[0][n] = wfrag(ra[n], 0);
    wfb[0][7] = wfrag(rc0, 0); wfb[0][8] = wfrag(rc1, 0);
    __builtin_amdgcn_sched_barrier(0);
    kstep(0); kstep(1); K1_WAIT(3, 48) kstep(2);
    kstep(3); kstep(4); K1_WAIT(6, 36) kstep(5);
    kstep(6); kstep(7); K1_WAIT(9, 24) kstep(8);
    kstep(9); kstep(10); K1_WAIT(12, 12) kstep(11);
    kstep(12); kstep(13); K1_WAIT(15, 0) kstep(14);
    kstep(15); kstep(16); kstep(17);
#undef K1_WAIT

    float* cs = (float*)(smem + 147456);
    if (SWAP) {
        if (i16 == 0) {
            *(f32x4*)(cs + 32 * w + 4 * g) = csacc[0];
            *(f32x4*)(cs + 32 * w + 16 + 4 * g) = csacc[1];
        }
    } else {
        if (g == 0) { cs[32 * w + i16] = csacc[0][0]; cs[32 * w + 16 + i16] = csacc[1][0]; }
    }
    float mean[2], rstd[2];
#pragma unroll
    for (int mi = 0; mi < 2; ++mi) {
        float a = sx[mi], b = sxx[mi];
        a += __shfl_xor(a, 16); b += __shfl_xor(b, 16);
        a += __shfl_xor(a, 32); b += __shfl_xor(b, 32);
        float m = a * (1.0f / 576.0f);
        float v = fmaxf(b * (1.0f / 576.0f) - m * m, 0.f);
        mean[mi] = m; rstd[mi] = rsqrtf(v + LN_EPS);
    }
    __syncthreads();
    const int off = (j & 1) * 4;
    const int tile = mt * 4 + w;
    char* kvb = ws + WS_KV;
    if (SWAP) {
        const float qs = 0.23570226039551584f * 1.4426950408889634f;
        const int tmain = (tile & 7) * 1024 + i16 * 16;
        const int textra = 8192 + (tile & 1) * 512 + ((tile & 7) >> 1) * 4 + i16 * 16;
#pragma unroll
        for (int n = 0; n < 7; ++n) {
            f32x4 c4 = *(const f32x4*)(cs + 16 * n + 4 * g);
#pragma unroll
            for (int pr = 0; pr < 2; ++pr) {
                const int col = 16 * n + 4 * g + 2 * pr - off;
                const bool valid = (col >= 0) && (col < 108);
                const int s = (col * 57) >> 10;
                const int d = col - 18 * s;
                const int gs = 6 * j + s;
                const int part = gs >> 5, head = gs & 31;
                const float sc = part == 0 ? qs : 1.0f;
                size_t base = part == 0 ? (WS_Q + (size_t)head * QIMG + (size_t)(tile >> 3) * QCH)
                                        : (WS_KV + (size_t)head * KVIMG + (size_t)(tile >> 3) * KCH);
                base += d < 16 ? (tmain + (d >> 3) * 512 + (d & 7) * 2) : textra;
#pragma unroll
                for (int mi = 0; mi < 2; ++mi) {
                    const float rs = rstd[mi] * sc;
                    float v0 = (acc[mi][n][2 * pr] - mean[mi] * c4[2 * pr]) * rs;
                    float v1 = (acc[mi][n][2 * pr + 1] - mean[mi] * c4[2 * pr + 1]) * rs;
                    unsigned pk = pk2(v0, v1);
                    if (valid) {
                        if (part < 2) {
                            *(unsigned*)(ws + base + mi * 256) = pk;
                        } else {
                            char* vp = kvb + (size_t)head * KVIMG + (size_t)(tile >> 3) * KCH + 9216 +
                                       ((tile & 7) * 2 + mi) * 608 + ((i16 >> 2) & 1) * 304 + d * 16 +
                                       (((i16 >> 3) << 2) | (i16 & 3)) * 2;
                            *(unsigned short*)vp = (unsigned short)(pk & 0xffff);
                            *(unsigned short*)(vp + 16) = (unsigned short)(pk >> 16);
                        }
                    }
                }
            }
        }
    } else {
        float mn[2][4], rs[2][4];
#pragma unroll
        for (int mi = 0; mi < 2; ++mi)
#pragma unroll
            for (int r = 0; r < 4; ++r) {
                mn[mi][r] = __shfl(mean[mi], 4 * g + r);
                rs[mi][r] = __shfl(rstd[mi], 4 * g + r);
            }
#pragma unroll
        for (int n = 0; n < 7; ++n) {
            const float c1 = cs[16 * n + i16];
            const int col = 16 * n + i16 - off;
            const bool valid = (col >= 0) && (col < 108);
            const int s = (col * 57) >> 10;
            const int d = col - 18 * s;
            const int head = (6 * j + s) & 31;
            char* vp = kvb + (size_t)head * KVIMG + (size_t)(tile >> 3) * KCH + 9216 + (tile & 7) * 1216 +
                       (g & 1) * 304 + d * 16 + (g >> 1) * 8;
#pragma unroll
            for (int mi = 0; mi < 2; ++mi) {
                u32x2 o;
                o[0] = pk2((acc[mi][n][0] - mn[mi][0] * c1) * rs[mi][0], (acc[mi][n][1] - mn[mi][1] * c1) * rs[mi][1]);
                o[1] = pk2((acc[mi][n][2] - mn[mi][2] * c1) * rs[mi][2], (acc[mi][n][3] - mn[mi][3] * c1) * rs[mi][3]);
                if (valid) *(u32x2*)(vp + mi * 608) = o;
            }
        }
    }
    if (j >= 10 && l < 24) {
        const int s = l >> 2, mi = (l >> 1) & 1, h = l & 1;
        const int gs = 6 * j + s;
        if (gs >= 64) {
            char* vp = kvb + (size_t)(gs & 31) * KVIMG + (size_t)(tile >> 3) * KCH + 9216 +
                       ((tile & 7) * 2 + mi) * 608 + h * 304 + 18 * 16;
            *(u32x4*)vp = (u32x4)0x3f803f80u;
        }
    }
}

__global__ __launch_bounds__(256, 1) void k1_qkv(const char* __restrict__ x, const char* __restrict__ wqkv,
                                                 char* __restrict__ ws) {
    __shared__ __attribute__((aligned(16))) char smem[147456 + 512];
    const int b = blockIdx.x, xcd = b & 7, i = b >> 3;
    const int mt = (xcd >> 1) * 4 + (i & 3), j = (xcd & 1) * 8 + (i >> 2);
    const int w = __builtin_amdgcn_readfirstlane(threadIdx.x >> 6), l = threadIdx.x & 63;
    if (j <= 10) k1_body<true>(smem, x, wqkv, ws, mt, j, w, l);
    else k1_body<false>(smem, x, wqkv, ws, mt, j, w, l);
}

#define SB0 __builtin_amdgcn_sched_barrier(0)

DI f32x16 lo16(const f32x32& v) { return __builtin_shufflevector(v, v, 0, 1, 2, 3, 4, 5, 6, 7, 8, 9, 10, 11, 12, 13, 14, 15); }
DI f32x16 hi16(const f32x32& v) { return __builtin_shufflevector(v, v, 16, 17, 18, 19, 20, 21, 22, 23, 24, 25, 26, 27, 28, 29, 30, 31); }

DI bf16x8 ldsb8(const char* p) { return *(const bf16x8*)p; }

DI bf16x8 exp_pack(const f32x16& s, int o) {
    u32x4 r;
#pragma unroll
    for (int e = 0; e < 4; ++e)
        r[e] = pk2(__builtin_amdgcn_exp2f(s[o + 2 * e]), __builtin_amdgcn_exp2f(s[o + 2 * e + 1]));
    return __builtin_bit_cast(bf16x8, r);
}

template <int PP>
DI void attn_step(const char* kcur, const char* knext, const char* vcur, f32x16& sa, f32x16& sb, f32x16& na, f32x16& nb,
                  f32x16& o, const bf16x8& q1, const s16x4& q2a, const s16x4& q2b, const f32x32& cinit, const u32x4& k2n) {
    s16x4 k2 = __builtin_bit_cast(s16x4, (PP & 2) ? __builtin_shufflevector(k2n, k2n, 2, 3) : __builtin_shufflevector(k2n, k2n, 0, 1));
    bf16x8 ka = ldsb8(knext), kb = ldsb8(knext + 1024);
    bf16x8 v0 = ldsb8(vcur), v1 = ldsb8(vcur + 608), v2 = ldsb8(vcur + 1216), v3 = ldsb8(vcur + 1824);
    (void)kcur;
    SB0;
    f32x32 n2 = __builtin_amdgcn_mfma_f32_32x32x4bf16_1k(k2, (PP & 1) ? q2b : q2a, cinit, 0, 0, 0);
    SB0;
    bf16x8 p0 = exp_pack(sa, 0);
    SB0;
    o = __builtin_amdgcn_mfma_f32_32x32x16_bf16(v0, p0, o, 0, 0, 0);
    SB0;
    bf16x8 p1 = exp_pack(sa, 8);
    SB0;
    na = __builtin_amdgcn_mfma_f32_32x32x16_bf16(ka, q1, lo16(n2), 0, 0, 0);
    o = __builtin_amdgcn_mfma_f32_32x32x16_bf16(v1, p1, o, 0, 0, 0);
    SB0;
    bf16x8 p2 = exp_pack(sb, 0);
    SB0;
    nb = __builtin_amdgcn_mfma_f32_32x32x16_bf16(kb, q1, hi16(n2), 0, 0, 0);
    o = __builtin_amdgcn_mfma_f32_32x32x16_bf16(v2, p2, o, 0, 0, 0);
    SB0;
    bf16x8 p3 = exp_pack(sb, 8);
    SB0;
    o = __builtin_amdgcn_mfma_f32_32x32x16_bf16(v3, p3, o, 0, 0, 0);
    SB0;
}

template <bool FIRST>
DI f32x16 attn_pass(const char* smem, int l, const bf16x8& q1, const s16x4& q2a, const s16x4& q2b, const f32x32& cinit) {
    const int r = l & 31, h = l >> 5;
    const char* kp = smem + h * 512 + r * 16;
    const char* vp = smem + 9216 + h * 304 + r * 16;
    f32x16 o = (f32x16)0.f;
    f32x16 sa, sb, ta, tb;
    u32x4 k2 = *(const u32x4*)(kp + 8192);
    {
        s16x4 k2p = __builtin_bit_cast(s16x4, __builtin_shufflevector(k2, k2, 0, 1));
        f32x32 n2 = __builtin_amdgcn_mfma_f32_32x32x4bf16_1k(k2p, q2a, cinit, 0, 0, 0);
        sa = __builtin_amdgcn_mfma_f32_32x32x16_bf16(ldsb8(kp), q1, lo16(n2), 0, 0, 0);
        sb = __builtin_amdgcn_mfma_f32_32x32x16_bf16(ldsb8(kp + 1024), q1, hi16(n2), 0, 0, 0);
    }
#pragma unroll 1
    for (int c = 0; c < 8; ++c) {
        attn_step<1>(kp, kp + 2048, vp, sa, sb, ta, tb, o, q1, q2a, q2b, cinit, k2);
        attn_step<2>(kp, kp + 4096, vp + 2432, ta, tb, sa, sb, o, q1, q2a, q2b, cinit, k2);
        attn_step<3>(kp, kp + 6144, vp + 4864, sa, sb, ta, tb, o, q1, q2a, q2b, cinit, k2);
        if (FIRST && c == 0) {
            asm volatile("s_waitcnt vmcnt(0)" ::: "memory");
            __builtin_amdgcn_s_barrier();
        }
        k2 = *(const u32x4*)(kp + KCH + 8192);
        attn_step<0>(kp, kp + KCH, vp + 7296, ta, tb, sa, sb, o, q1, q2a, q2b, cinit, k2);
        kp += KCH; vp += KCH;
    }
    return o;
}

__global__ __launch_bounds__(512, 2) void k2_attn(const unsigned short* __restrict__ wo, char* __restrict__ ws) {
    __shared__ __attribute__((aligned(16))) char smem[163840];
    const int b = blockIdx.x, xcd = b & 7, i = b >> 3;
    const int head = xcd * 4 + (i >> 3), qb = i & 7;
    const int w = __builtin_amdgcn_readfirstlane(threadIdx.x >> 6), l = threadIdx.x & 63;
    const int r = l & 31, h = l >> 5;
    const int qt = qb * 8 + w;

    const int fr = b * 8 + w;
    if (fr < 648) {
        const int nt = fr / 18, ks = fr - nt * 18;
        const int n = nt * 16 + (l & 15), k0 = ks * 32 + (l >> 4) * 8;
        unsigned short v[8];
#pragma unroll
        for (int e = 0; e < 8; ++e) {
            const int kk = k0 + e;
            const int row = kk < 512 ? ((kk >> 4) * 18 + (kk & 15)) : (((kk - 512) >> 1) * 18 + 16 + (kk & 1));
            v[e] = wo[row * 576 + n];
        }
        u32x4 o;
#pragma unroll
        for (int e = 0; e < 4; ++e) o[e] = (unsigned)v[2 * e] | ((unsigned)v[2 * e + 1] << 16);
        *(u32x4*)(ws + WS_WO + (size_t)fr * 1024 + l * 16) = o;
    }
    const char* qp = ws + WS_Q + (size_t)head * QIMG + (size_t)qb * QCH;
    bf16x8 q1 = *(const bf16x8*)(qp + w * 1024 + h * 512 + r * 16);
    unsigned q2 = *(const unsigned*)(qp + 8192 + (w & 1) * 512 + r * 16 + (w >> 1) * 4);
    asm volatile("s_waitcnt vmcnt(0)" : "+v"(q1), "+v"(q2) :: "memory");
    {
        const char* src = ws + WS_KV + (size_t)head * KVIMG + w * 1024 + l * 16;
        char* dst = smem + w * 1024;
#pragma unroll
        for (int t = 0; t < 19; ++t) glds16(src + t * 8192, dst + t * 8192);
    }
    u32x2 qa; qa[0] = q2; qa[1] = 0u;
    u32x2 qbv; qbv[0] = 0u; qbv[1] = q2;
    const s16x4 q2a = __builtin_bit_cast(s16x4, qa), q2b = __builtin_bit_cast(s16x4, qbv);
    f32x32 cinit = (f32x32)0.f;
    asm volatile("s_waitcnt vmcnt(16)" ::: "memory");
    __builtin_amdgcn_s_barrier();
    SB0;
    f32x16 o = attn_pass<true>(smem, l, q1, q2a, q2b, cinit);
    float lsum = __shfl(o[10], r);
    bool ok = (lsum >= 9.0e-13f) && (lsum <= 1.0e18f);
#pragma unroll
    for (int e = 0; e < 8; ++e) ok = ok && (fabsf(o[e]) <= 3.0e38f);
    ok = ok && (h == 1 || ((fabsf(o[8]) <= 3.0e38f) && (fabsf(o[9]) <= 3.0e38f)));
    if (!__all(ok)) {
        const char* kp = smem + h * 512 + r * 16;
        float m = -3.0e38f;
#pragma unroll 1
        for (int c = 0; c < 8; ++c) {
            u32x4 k2 = *(const u32x4*)(kp + 8192);
#pragma unroll
            for (int pp = 0; pp < 4; ++pp) {
                s16x4 k2p = __builtin_bit_cast(s16x4, (pp & 2) ? __builtin_shufflevector(k2, k2, 2, 3) : __builtin_shufflevector(k2, k2, 0, 1));
                f32x32 n2 = __builtin_amdgcn_mfma_f32_32x32x4bf16_1k(k2p, (pp & 1) ? q2b : q2a, cinit, 0, 0, 0);
                f32x16 sa = __builtin_amdgcn_mfma_f32_32x32x16_bf16(ldsb8(kp + pp * 2048), q1, lo16(n2), 0, 0, 0);
                f32x16 sb = __builtin_amdgcn_mfma_f32_32x32x16_bf16(ldsb8(kp + pp * 2048 + 1024), q1, hi16(n2), 0, 0, 0);
#pragma unroll
                for (int e = 0; e < 16; ++e) m = fmaxf(m, fmaxf(sa[e], sb[e]));
            }
            kp += KCH;
        }
        m = fmaxf(m, __shfl_xor(m, 32));
        cinit = (f32x32)(-m);
        o = attn_pass<false>(smem, l, q1, q2a, q2b, cinit);
        lsum = __shfl(o[10], r);
    }
    const float inv = 1.0f / lsum;
    char* op = ws + WS_O + ((size_t)(qt * 2 + (r >> 4)) * 18) * 1024;
    {
        u32x2 a, c;
        a[0] = pk2(o[0] * inv, o[1] * inv); a[1] = pk2(o[2] * inv, o[3] * inv);
        c[0] = pk2(o[4] * inv, o[5] * inv); c[1] = pk2(o[6] * inv, o[7] * inv);
        char* mp = op + (head >> 1) * 1024 + (head & 1) * 512 + (r & 15) * 16 + h * 8;
        *(u32x2*)mp = a;
        *(u32x2*)(mp + 256) = c;
        if (h == 0)
            *(unsigned*)(op + (16 + (head >> 4)) * 1024 + ((head >> 2) & 3) * 256 + (r & 15) * 16 + (head & 3) * 4) =
                pk2(o[8] * inv, o[9] * inv);
    }
}

__global__ __launch_bounds__(576, 1) void k3_oproj(const char* __restrict__ x, const char* __restrict__ wr,
                                                   char* __restrict__ ws) {
    __shared__ __attribute__((aligned(16))) char smem[36864 + 9 * 32 * 3 * 4];
    const int b = blockIdx.x, xcd = b & 7, i = b >> 3;
    const int mb = xcd * 8 + (i >> 2), ns = i & 3;
    const int w = __builtin_amdgcn_readfirstlane(threadIdx.x >> 6), l = threadIdx.x & 63;
    const int g = l >> 4, i16 = l & 15;
    const int nt = ns * 9 + w;
    const char* osrc = ws + WS_O + (size_t)mb * 36864 + w * 1024 + l * 16;
    u32x4 ot[4];
#pragma unroll
    for (int t = 0; t < 4; ++t) ot[t] = *(const u32x4*)(osrc + t * 9216);
    const char* wsrc = ws + WS_WO + (size_t)nt * 18432 + l * 16;
    bf16x8 wf[18];
#pragma unroll
    for (int ks = 0; ks < 18; ++ks) wf[ks] = *(const bf16x8*)(wsrc + ks * 1024);
    const int c0 = nt * 16 + 4 * g;
    u32x2 xr[2];
#pragma unroll
    for (int mi = 0; mi < 2; ++mi) xr[mi] = *(const u32x2*)(x + (size_t)(mb * 32 + mi * 16 + i16) * 1152 + c0 * 2);
    u32x2 wrv = *(const u32x2*)(wr + c0 * 2);
#pragma unroll
    for (int t = 0; t < 4; ++t) *(u32x4*)(smem + w * 1024 + l * 16 + t * 9216) = ot[t];
    __syncthreads();
    f32x4 acc[2] = {(f32x4)0.f, (f32x4)0.f};
#pragma unroll
    for (int ks = 0; ks < 18; ++ks) {
#pragma unroll
        for (int mi = 0; mi < 2; ++mi) {
            bf16x8 of = *(const bf16x8*)(smem + (mi * 18 + ks) * 1024 + l * 16);
            acc[mi] = __builtin_amdgcn_mfma_f32_16x16x32_bf16(wf[ks], of, acc[mi], 0, 0, 0);
        }
    }
    float* red = (float*)(smem + 36864);
    const float w0 = bflo(wrv[0]), w1 = bfhi(wrv[0]), w2 = bflo(wrv[1]), w3 = bfhi(wrv[1]);
#pragma unroll
    for (int mi = 0; mi < 2; ++mi) {
        u32x2 hv;
        hv[0] = pk2(bflo(xr[mi][0]) + rbf(acc[mi][0]), bfhi(xr[mi][0]) + rbf(acc[mi][1]));
        hv[1] = pk2(bflo(xr[mi][1]) + rbf(acc[mi][2]), bfhi(xr[mi][1]) + rbf(acc[mi][3]));
        *(u32x2*)(ws + WS_H + (size_t)(mb * 32 + mi * 16 + i16) * 1152 + c0 * 2) = hv;
        const float h0 = bflo(hv[0]), h1 = bfhi(hv[0]), h2 = bflo(hv[1]), h3 = bfhi(hv[1]);
        float s = (h0 + h1) + (h2 + h3);
        float qq = (h0 * h0 + h1 * h1) + (h2 * h2 + h3 * h3);
        float e = (h0 * w0 + h1 * w1) + (h2 * w2 + h3 * w3);
        s += __shfl_xor(s, 16); qq += __shfl_xor(qq, 16); e += __shfl_xor(e, 16);
        s += __shfl_xor(s, 32); qq += __shfl_xor(qq, 32); e += __shfl_xor(e, 32);
        if (g == 0) {
            red[(w * 3 + 0) * 32 + mi * 16 + i16] = s;
            red[(w * 3 + 1) * 32 + mi * 16 + i16] = qq;
            red[(w * 3 + 2) * 32 + mi * 16 + i16] = e;
        }
    }
    __syncthreads();
    if (threadIdx.x < 96) {
        const int which = threadIdx.x >> 5, tk = threadIdx.x & 31;
        float a = 0.f;
#pragma unroll
        for (int ww = 0; ww < 9; ++ww) a += red[(ww * 3 + which) * 32 + tk];
        ((float*)(ws + WS_ST))[(which * 4 + ns) * S_ + mb * 32 + tk] = a;
    }
}

__global__ __launch_bounds__(576, 1) void k4_ffn(const char* __restrict__ wr, const char* __restrict__ wg,
                                                 const char* __restrict__ wu, const char* __restrict__ wd,
                                                 char* __restrict__ ws, char* __restrict__ out) {
    __shared__ __attribute__((aligned(16))) float sm[8 * 576 + 64];
    const int tid = threadIdx.x;
    const float* st = (const float*)(ws + WS_ST);
    if (blockIdx.x >= 144) {
        const int rb = (blockIdx.x - 144) * 16;
        const int rr = tid / 72, ch = tid - rr * 72;
#pragma unroll
        for (int ps = 0; ps < 2; ++ps) {
            const int row = rb + ps * 8 + rr;
            u32x4 hv = *(const u32x4*)(ws + WS_H + (size_t)row * 1152 + ch * 16);
            float s = (st[0 * S_ + row] + st[1 * S_ + row]) + (st[2 * S_ + row] + st[3 * S_ + row]);
            float qq = (st[4 * S_ + row] + st[5 * S_ + row]) + (st[6 * S_ + row] + st[7 * S_ + row]);
            const float mean = s * (1.0f / 576.0f);
            const float rstd = rsqrtf(fmaxf(qq * (1.0f / 576.0f) - mean * mean, 0.f) + LN_EPS);
            u32x4 ov;
#pragma unroll
            for (int e = 0; e < 4; ++e) {
                const float a = bflo(hv[e]), c = bfhi(hv[e]);
                ov[e] = pk2(a + rbf((a - mean) * rstd), c + rbf((c - mean) * rstd));
            }
            *(u32x4*)(out + (size_t)row * 1152 + ch * 16) = ov;
        }
        return;
    }
    const int bx = blockIdx.x;
    const int f = (bx & 7) * 18 + (bx >> 3);
    const int w = tid >> 6, l = tid & 63;
    const int kr = tid >> 2, cq = tid & 3;
    u32x4 gv[4], uv[4], dv[4];
#pragma unroll
    for (int t = 0; t < 4; ++t) {
        gv[t] = *(const u32x4*)(wg + (size_t)(kr + 144 * t) * (F_ * 2) + f * 64 + cq * 16);
        uv[t] = *(const u32x4*)(wu + (size_t)(kr + 144 * t) * (F_ * 2) + f * 64 + cq * 16);
    }
    const int dr = tid / 72, dch = tid - dr * 72;
#pragma unroll
    for (int t = 0; t < 4; ++t) dv[t] = *(const u32x4*)(wd + (size_t)(f * 32 + dr + 8 * t) * 1152 + dch * 16);
    float wsum = bf2f(((const unsigned short*)wr)[tid]);
    float best = -3.4e38f, bmean = 0.f, brstd = 0.f;
    int bidx = 0x7fffffff;
    f32x4 sv[12];
    if (tid < 512) {
#pragma unroll
        for (int a = 0; a < 12; ++a) sv[a] = *(const f32x4*)(st + a * S_ + tid * 4);
    }
#pragma unroll
    for (int o = 32; o >= 1; o >>= 1) wsum += __shfl_xor(wsum, o);
    if (l == 0) sm[4608 + w] = wsum;
    __syncthreads();
    wsum = 0.f;
#pragma unroll
    for (int ww = 0; ww < 9; ++ww) wsum += sm[4608 + ww];
    if (tid < 512) {
#pragma unroll
        for (int e = 0; e < 4; ++e) {
            const float s = (sv[0][e] + sv[1][e]) + (sv[2][e] + sv[3][e]);
            const float qq = (sv[4][e] + sv[5][e]) + (sv[6][e] + sv[7][e]);
            const float ee = (sv[8][e] + sv[9][e]) + (sv[10][e] + sv[11][e]);
            const float mean = s * (1.0f / 576.0f);
            const float rstd = rsqrtf(fmaxf(qq * (1.0f / 576.0f) - mean * mean, 0.f) + LN_EPS);
            const float rv = (ee - mean * wsum) * rstd;
            if (rv > best || bidx == 0x7fffffff) { best = rv; bidx = tid * 4 + e; bmean = mean; brstd = rstd; }
        }
    }
#pragma unroll
    for (int o = 32; o >= 1; o >>= 1) {
        const float ob = __shfl_xor(best, o); const int oi = __shfl_xor(bidx, o);
        const float om = __shfl_xor(bmean, o), os = __shfl_xor(brstd, o);
        if (ob > best || (ob == best && oi < bidx) || (bidx == 0x7fffffff)) { best = ob; bidx = oi; bmean = om; brstd = os; }
    }
    if (l == 0 && w < 8) { sm[4624 + 4 * w] = best; sm[4625 + 4 * w] = __builtin_bit_cast(float, bidx); sm[4626 + 4 * w] = bmean; sm[4627 + 4 * w] = brstd; }
    __syncthreads();
    best = sm[4624]; bidx = __builtin_bit_cast(int, sm[4625]); bmean = sm[4626]; brstd = sm[4627];
#pragma unroll
    for (int ww = 1; ww < 8; ++ww) {
        const float ob = sm[4624 + 4 * ww];
        if (ob > best) { best = ob; bidx = __builtin_bit_cast(int, sm[4625 + 4 * ww]); bmean = sm[4626 + 4 * ww]; brstd = sm[4627 + 4 * ww]; }
    }
    bidx = bidx & (S_ - 1);
    if (bx == 0 && tid == 0) {
        float* sel = (float*)(ws + WS_SEL);
        sel[0] = __builtin_bit_cast(float, bidx); sel[1] = bmean; sel[2] = brstd; sel[3] = best;
    }
    const unsigned short* hrow = (const unsigned short*)(ws + WS_H + (size_t)bidx * 1152);
    float ga[8], ua[8];
#pragma unroll
    for (int e = 0; e < 8; ++e) { ga[e] = 0.f; ua[e] = 0.f; }
    float xs[4];
#pragma unroll
    for (int t = 0; t < 4; ++t) xs[t] = rbf((bf2f(hrow[kr + 144 * t]) - bmean) * brstd);
#pragma unroll
    for (int t = 0; t < 4; ++t)
#pragma unroll
        for (int e = 0; e < 4; ++e) {
            ga[2 * e] += xs[t] * bflo(gv[t][e]); ga[2 * e + 1] += xs[t] * bfhi(gv[t][e]);
            ua[2 * e] += xs[t] * bflo(uv[t][e]); ua[2 * e + 1] += xs[t] * bfhi(uv[t][e]);
        }
#pragma unroll
    for (int e = 0; e < 8; ++e) {
        ga[e] += __shfl_xor(ga[e], 4); ua[e] += __shfl_xor(ua[e], 4);
        ga[e] += __shfl_xor(ga[e], 8); ua[e] += __shfl_xor(ua[e], 8);
    }
    if ((l & 15) < 4) {
        float* dst = sm + (w * 4 + (l >> 4)) * 64 + cq * 8;
        *(f32x4*)(dst) = (f32x4){ga[0], ga[1], ga[2], ga[3]}; *(f32x4*)(dst + 4) = (f32x4){ga[4], ga[5], ga[6], ga[7]};
        *(f32x4*)(dst + 32) = (f32x4){ua[0], ua[1], ua[2], ua[3]}; *(f32x4*)(dst + 36) = (f32x4){ua[4], ua[5], ua[6], ua[7]};
    }
    __syncthreads();
    if (tid < 64) {
        float a = 0.f;
#pragma unroll
        for (int p = 0; p < 36; ++p) a += sm[p * 64 + tid];
        a = rbf(a);
        const float other = __shfl_xor(a, 32);
        const float sg = rbf(a / (1.0f + __expf(-a)));
        if (tid < 32) sm[4640 + tid] = rbf(sg * other);
    }
    __syncthreads();
    float ya[8];
#pragma unroll
    for (int e = 0; e < 8; ++e) ya[e] = 0.f;
#pragma unroll
    for (int t = 0; t < 4; ++t) {
        const float a = sm[4640 + dr + 8 * t];
#pragma unroll
        for (int e = 0; e < 4; ++e) { ya[2 * e] += a * bflo(dv[t][e]); ya[2 * e + 1] += a * bfhi(dv[t][e]); }
    }
    *(f32x4*)(sm + dr * 576 + dch * 8) = (f32x4){ya[0], ya[1], ya[2], ya[3]};
    *(f32x4*)(sm + dr * 576 + dch * 8 + 4) = (f32x4){ya[4], ya[5], ya[6], ya[7]};
    __syncthreads();
    float y = 0.f;
#pragma unroll
    for (int p = 0; p < 8; ++p) y += sm[p * 576 + tid];
    ((float*)(ws + WS_YP))[f * 576 + tid] = y;
}

__global__ __launch_bounds__(256) void k5_fin(const char* __restrict__ ws, char* __restrict__ out) {
    __shared__ float sm[256];
    const int tid = threadIdx.x, c = tid & 15, p = tid >> 4;
    const int col = blockIdx.x * 16 + c;
    const float* yp = (const float*)(ws + WS_YP);
    const f32x4 sel = *(const f32x4*)(ws + WS_SEL);
    float a = 0.f;
#pragma unroll
    for (int t = 0; t < 9; ++t) a += yp[(p + 16 * t) * 576 + col];
    const int idx = __builtin_bit_cast(int, sel[0]);
    const float hv = bf2f(((const unsigned short*)(ws + WS_H))[(size_t)idx * 576 + col]);
    sm[tid] = a;
    __syncthreads();
    if (tid < 16) {
        float y = 0.f;
#pragma unroll
        for (int t = 0; t < 16; ++t) y += sm[t * 16 + tid];
        const float rsel = rbf(sel[3]);
        const float sg = rbf(1.0f / (1.0f + __expf(-rsel)));
        const float hn = rbf((hv - sel[1]) * sel[2]);
        const float upd = rbf(rbf(y) * sg);
        __bf16 o = (__bf16)(hv + rbf(hn + upd));
        ((unsigned short*)out)[(size_t)idx * 576 + col] = __builtin_bit_cast(unsigned short, o);
    }
}

extern "C" void kernel_launch(void* const* d_in, const int* in_sizes, int n_in,
                              void* d_out, int out_size, void* d_ws, size_t ws_size,
                              hipStream_t stream) {
    const char* x = (const char*)d_in[0];
    const char* wqkv = (const char*)d_in[1];
    const unsigned short* wo = (const unsigned short*)d_in[2];
    const char* wr = (const char*)d_in[4];
    const char* wg = (const char*)d_in[5];
    const char* wu = (const char*)d_in[6];
    const char* wd = (const char*)d_in[7];
    char* ws = (char*)d_ws;
    char* out = (char*)d_out;
    k1_qkv<<<256, 256, 0, stream>>>(x, wqkv, ws);
    k2_attn<<<256, 512, 0, stream>>>(wo, ws);
    k3_oproj<<<256, 576, 0, stream>>>(x, wr, ws);
    k4_ffn<<<272, 576, 0, stream>>>(wr, wg, wu, wd, ws, out);
    k5_fin<<<36, 256, 0, stream>>>(ws, out);
}
```

```cpp
#include <hip/hip_runtime.h>
#include <hip/hip_bf16.h>
#include <stdint.h>

typedef __attribute__((ext_vector_type(4))) float f32x4;
typedef __attribute__((ext_vector_type(16))) float f32x16;
typedef __attribute__((ext_vector_type(32))) float f32x32;
typedef __attribute__((ext_vector_type(8))) __bf16 bf16x8;
typedef __attribute__((ext_vector_type(2))) __bf16 bf16x2;
typedef __attribute__((ext_vector_type(4))) short s16x4;
typedef __attribute__((ext_vector_type(8))) short s16x8;
typedef __attribute__((ext_vector_type(4))) unsigned u32x4;
typedef __attribute__((ext_vector_type(2))) unsigned u32x2;
typedef __attribute__((address_space(3))) s16x4* lds_s16x4_ptr;

#define DI __device__ __forceinline__

#define STAMP(kid, i)
#define STAMPC(kid, i)

constexpr int S_ = 2048, D_ = 576, F_ = 4608;
constexpr float LN_EPS = 1e-5f;

constexpr int QCH = 9216;
constexpr int KCH = 18944;
constexpr int QIMG = 8 * QCH;
constexpr int KVIMG = 155648;
constexpr size_t WS_Q = 0;
constexpr size_t WS_KV = WS_Q + (size_t)32 * QIMG;
constexpr size_t WS_O = WS_KV + (size_t)32 * KVIMG;
constexpr size_t WS_WO = WS_O + (size_t)128 * 18 * 1024;
constexpr size_t WS_H = WS_WO + (size_t)36 * 18 * 1024;
constexpr size_t WS_GR = WS_H + (size_t)S_ * D_ * 2;
constexpr size_t WS_GR3 = WS_GR;
constexpr size_t WS_GR4 = WS_GR + (size_t)256 * 768;
constexpr size_t WS_CAND = WS_GR + (1u << 20);

typedef __attribute__((address_space(1))) unsigned long long gu64;
#define RLX_AGENT __ATOMIC_RELAXED, __HIP_MEMORY_SCOPE_AGENT
constexpr unsigned SPIN_CAP = 1u << 20;
DI void put_granule(char* p, float v) {
    __hip_atomic_store((gu64*)p, (1ull << 32) | __builtin_bit_cast(unsigned, v), RLX_AGENT);
}
template <int N>
DI void get_granules(const char* p, int stride, float (&v)[N]) {
    for (unsigned spins = 0; spins < SPIN_CAP; ++spins) {
        bool ok = true;
#pragma unroll
        for (int k = 0; k < N; ++k) {
            const unsigned long long x = __hip_atomic_load((gu64*)(p + k * stride), RLX_AGENT);
            v[k] = __builtin_bit_cast(float, (unsigned)x); ok &= (x >> 32) == 1ull;
        }
        if (__all(ok)) break;
        __builtin_amdgcn_s_sleep(1);
    }
}
DI float sw32(float a, float b) {
    auto r = __builtin_amdgcn_permlane32_swap(__builtin_bit_cast(unsigned, a), __builtin_bit_cast(unsigned, b), false, false);
    const unsigned r0 = r[0], r1 = r[1];
    return __builtin_bit_cast(float, r0) + __builtin_bit_cast(float, r1);
}
DI float sw16(float a, float b) {
    auto r = __builtin_amdgcn_permlane16_swap(__builtin_bit_cast(unsigned, a), __builtin_bit_cast(unsigned, b), false, false);
    const unsigned r0 = r[0], r1 = r[1];
    return __builtin_bit_cast(float, r0) + __builtin_bit_cast(float, r1);
}
template <int R> DI unsigned ror(unsigned v) { return (unsigned)__builtin_amdgcn_update_dpp(0, (int)v, 0x120 + R, 0xf, 0xf, false); }
template <int R> DI float rorf(float v) { return __builtin_bit_cast(float, ror<R>(__builtin_bit_cast(unsigned, v))); }
DI int argmax_lane(float v) {
    const unsigned u = __builtin_bit_cast(unsigned, v);
    const unsigned key = (u & 0x80000000u) ? ~u : (u | 0x80000000u);
    unsigned m = key;
    m = max(m, ror<1>(m)); m = max(m, ror<2>(m)); m = max(m, ror<4>(m)); m = max(m, ror<8>(m));
    { auto r = __builtin_amdgcn_permlane16_swap(m, m, false, false); const unsigned r0 = r[0], r1 = r[1]; m = max(r0, r1); }
    { auto r = __builtin_amdgcn_permlane32_swap(m, m, false, false); const unsigned r0 = r[0], r1 = r[1]; m = max(r0, r1); }
    return __builtin_ctzll(__ballot(key == m));
}

DI float bf2f(unsigned short v) { return __builtin_bit_cast(float, (unsigned)v << 16); }
DI float bflo(unsigned v) { return __builtin_bit_cast(float, v << 16); }
DI float bfhi(unsigned v) { return __builtin_bit_cast(float, v & 0xffff0000u); }
DI unsigned pk2(float a, float b) {
    bf16x2 r; r[0] = (__bf16)a; r[1] = (__bf16)b;
    return __builtin_bit_cast(unsigned, r);
}
DI float rbf(float a) { return (float)(__bf16)a; }

#define WT " sc1"
DI void st16wt(void* p, const u32x4& v) { asm volatile("global_store_dwordx4 %0, %1, off" WT "\n\ts_nop 2" :: "v"(p), "v"(v) : "memory"); }
DI void st8wt(void* p, const u32x2& v) { asm volatile("global_store_dwordx2 %0, %1, off" WT "\n\ts_nop 2" :: "v"(p), "v"(v) : "memory"); }
DI void st4wt(void* p, unsigned v) { asm volatile("global_store_dword %0, %1, off" WT :: "v"(p), "v"(v) : "memory"); }

DI void glds16(const void* g, void* lds) {
    __builtin_amdgcn_global_load_lds((const __attribute__((address_space(1))) unsigned*)g,
                                     (__attribute__((address_space(3))) unsigned*)lds, 16, 0, 0);
}

constexpr int K1_EPI = 131072;
constexpr int K1_CS = K1_EPI + 6 * 4864;
constexpr int K1_ST = K1_CS + 512;
constexpr int K1_DUM = K1_ST + 1024;
constexpr int K1_LDS = 163840;

struct K1Frag { bf16x8 x[2][2]; bf16x8 w[2][5]; };
typedef __attribute__((address_space(3))) unsigned* lds_u32;
typedef __attribute__((address_space(3))) unsigned short* lds_u16;
typedef __attribute__((address_space(3))) u32x2* lds_u32x2;

template <int MODE, int NH>
DI void k1_body(char* smem, char* __restrict__ ws, int mt, int j, int w, int l) {
    constexpr bool SWAP = MODE != 2;
    constexpr int NW = NH ? 5 : 4, NN = NH ? 3 : 4;
    const int g = l >> 4, i16 = l & 15;
    const int mw = w & 3;
    STAMP(0, 0);
    f32x4 acc[2][4], asq = (f32x4)0.f;
#pragma unroll
    for (int n = 0; n < 4; ++n) { acc[0][n] = (f32x4)0.f; acc[1][n] = (f32x4)0.f; }
    bf16x8 ones;
#pragma unroll
    for (int e = 0; e < 8; ++e) ones[e] = (__bf16)1.0f;

    const int q = i16 >> 2, p = i16 & 3;
    const int fl = q | ((g & 1) << 2);
    const unsigned sbase = (unsigned)(uintptr_t)smem;
    unsigned xa[2];
    xa[0] = sbase + mw * 4096 + i16 * 128 + ((g ^ (i16 >> 1)) << 4); xa[1] = xa[0] ^ 64;
    const unsigned wrow = sbase + 16384 + (8 * g + q) * 256 + p * 8;
    unsigned wa[NW];
#pragma unroll
    for (int n = 0; n < NN; ++n) wa[n] = wrow + (((4 * NH + n) ^ fl) << 5);
    if (NH) { wa[3] = wrow + (((2 * mw) ^ fl) << 5); wa[NW - 1] = wrow + (((2 * mw + 1) ^ fl) << 5); }
    const int odd = j & 1, off = odd * 4;
    const bool patch = NH ? (!odd && i16 >= 12) : (odd && i16 < 4);

    const int tid = w * 64 + l;
    unsigned ea[NN][2], ev[NN][2]; float es[NN][2];
    const unsigned epi = sbase + K1_EPI, dum = sbase + K1_DUM + l * 8;
#pragma unroll
    for (int n = 0; n < NN; ++n) {
#pragma unroll
        for (int pr = 0; pr < (SWAP ? 2 : 1); ++pr) {
            const int col = SWAP ? 16 * (4 * NH + n) + 4 * g + 2 * pr - off : 16 * (4 * NH + n) + i16 - off;
            const bool valid = (col >= 0) && (col < 108);
            const int sI = (col * 57) >> 10;
            const int d = col - 18 * sI;
            const int gs = 6 * j + sI;
            if (SWAP) {
                const unsigned qk = epi + sI * 4864 + i16 * 16 + (d < 16 ? mw * 1024 + (d >> 3) * 512 + (d & 7) * 2 : 4096 + mw * 4);
                ea[n][pr] = (valid && gs < 64) ? qk : dum;
                asm volatile("" : "+v"(ea[n][pr]));
                if (MODE == 0) {
                    es[n][pr] = gs < 32 ? 0.23570226039551584f * 1.4426950408889634f : 1.0f;
                    asm volatile("" : "+v"(es[n][pr]));
                } else {
                    const unsigned vv = epi + sI * 4864 + mw * 1216 + ((i16 >> 2) & 1) * 304 + d * 16 + (((i16 >> 3) << 2) | (i16 & 3)) * 2;
                    ev[n][pr] = (valid && gs >= 64) ? vv : dum;
                    asm volatile("" : "+v"(ev[n][pr]));
                }
            } else {
                const unsigned vv = epi + sI * 4864 + mw * 1216 + (g & 1) * 304 + d * 16 + (g >> 1) * 8;
                ea[n][0] = valid ? vv : dum;
                asm volatile("" : "+v"(ea[n][0]));
            }
        }
    }
    int go[4];
    const int ch = mt >> 1, hb = mt & 1;
#pragma unroll
    for (int t = 0; t < 4; ++t) {
        const int un = tid + 512 * t;
        const int sI = (un * 3450) >> 20;
        const int o = (un - sI * 304) * 16;
        const int gs = 6 * j + sI, part = gs >> 5, head = gs & 31;
        const int base = part == 0 ? (int)WS_Q + head * QIMG + ch * QCH : (int)WS_KV + head * KVIMG + ch * KCH;
        const int dst = part == 2 ? 9216 + hb * 4864 + o : (o < 4096 ? hb * 4096 + o : 4096 + hb * 512 + o);
        go[t] = (un < 6 * 304 && (part == 2 || o < 4608)) ? base + dst : -1;
        asm volatile("" : "+v"(go[t]));
    }
    if (MODE != 0 && tid < 96 && 6 * j + (tid >> 4) >= 64)
        *(u32x4*)(smem + K1_EPI + (tid >> 4) * 4864 + ((tid >> 1) & 7) * 608 + (tid & 1) * 304 + 288) = (u32x4)0x3f803f80u;

    typedef __attribute__((address_space(3))) const bf16x8* lds_b8;
    auto rd = [&](K1Frag& f, unsigned so, int i) {
        const int kk = i / (NW + 1), t = i % (NW + 1);
        if (t == 0) {
            f.x[kk][0] = *(lds_b8)(xa[kk] + so);
            f.x[kk][1] = *(lds_b8)(xa[kk] + so + 2048);
        } else {
            s16x4 lo = __builtin_amdgcn_ds_read_tr16_b64_v4i16((lds_s16x4_ptr)(wa[t - 1] + so + kk * 8192));
            s16x4 hi = __builtin_amdgcn_ds_read_tr16_b64_v4i16((lds_s16x4_ptr)(wa[t - 1] + so + kk * 8192 + 1024));
            f.w[kk][t - 1] = __builtin_bit_cast(bf16x8, __builtin_shufflevector(lo, hi, 0, 1, 2, 3, 4, 5, 6, 7));
        }
    };
    auto mm = [&](const bf16x8& wf, const bf16x8& xf, f32x4& c) {
        c = SWAP ? __builtin_amdgcn_mfma_f32_16x16x32_bf16(wf, xf, c, 0, 0, 0)
                 : __builtin_amdgcn_mfma_f32_16x16x32_bf16(xf, wf, c, 0, 0, 0);
    };
    auto compute = [&](const K1Frag& cur, K1Frag& nxt, unsigned so, bool pf) {
        constexpr int PN = NH ? 2 : 0;
        bf16x8 wp[2];
#pragma unroll
        for (int m = 0; m < 18; ++m) {
            const int kk = m / 9, t = m % 9;
            if (t == 0) mm(cur.x[kk][NH], cur.x[kk][NH], asq);
            else if (t >= 7) mm(wp[kk], cur.x[kk][t - 7], acc[t - 7][PN]);
            else if (NH == 0) mm(cur.w[kk][(t + 1) >> 1], cur.x[kk][(t + 1) & 1], acc[(t + 1) & 1][(t + 1) >> 1]);
            else if (t <= 2) mm(cur.w[kk][t + 2], ones, acc[t - 1][3]);
            else mm(cur.w[kk][(t - 3) >> 1], cur.x[kk][(t - 3) & 1], acc[(t - 3) & 1][(t - 3) >> 1]);
            __builtin_amdgcn_sched_barrier(0);
            if (pf && m < 2 * (NW + 1)) rd(nxt, so, m);
            if (m >= 12 - 12 * (1 - kk) + 2 && m < 12 - 12 * (1 - kk) + 3) {
                u32x4 v = __builtin_bit_cast(u32x4, cur.w[kk][PN]);
#pragma unroll
                for (int e = 0; e < 4; ++e) v[e] = patch ? 0x3f803f80u : v[e];
                wp[kk] = __builtin_bit_cast(bf16x8, v);
            }
            __builtin_amdgcn_sched_barrier(0);
        }
    };
    K1Frag fa, fb;
    __builtin_amdgcn_s_barrier();
    __builtin_amdgcn_sched_barrier(0);
    STAMP(0, 2);
    STAMPC(0, 14);
#pragma unroll
    for (int i = 0; i < 2 * (NW + 1); ++i) rd(fa, 0, i);
#pragma unroll 1
    for (int it = 0; it < 8; it += 2) {
        asm volatile("s_waitcnt lgkmcnt(0)" ::: "memory");
        __builtin_amdgcn_s_barrier();
        __builtin_amdgcn_sched_barrier(0);
        compute(fa, fb, 32768, true);
        xa[0] ^= 65536; xa[1] ^= 65536;
#pragma unroll
        for (int n = 0; n < NW; ++n) wa[n] ^= 65536;
        asm volatile("s_waitcnt lgkmcnt(0)" ::: "memory");
        __builtin_amdgcn_s_barrier();
        __builtin_amdgcn_sched_barrier(0);
        compute(fb, fa, 0, true);
    }
    compute(fa, fb, 0, false);
    STAMPC(0, 15);
    STAMP(0, 3);
    STAMP(0, 4);

    float* cs = (float*)(smem + K1_CS);
    float* sx = (float*)(smem + K1_ST);
    if (NH == 1) {
        if (SWAP) {
            if (i16 == 0) { *(f32x4*)(cs + 32 * mw + 4 * g) = acc[0][3]; *(f32x4*)(cs + 32 * mw + 16 + 4 * g) = acc[1][3]; }
        } else {
            if (g == 0) { cs[32 * mw + i16] = acc[0][3][0]; cs[32 * mw + 16 + i16] = acc[1][3][0]; }
        }
    }
    if (odd != NH) {
        constexpr int PN = NH ? 2 : 0;
        if (SWAP) {
            if (g == (NH ? 3 : 0)) { sx[32 * mw + i16] = acc[0][PN][0]; sx[32 * mw + 16 + i16] = acc[1][PN][0]; }
        } else {
            if (i16 == (NH ? 12 : 0)) { *(f32x4*)(sx + 32 * mw + 4 * g) = acc[0][PN]; *(f32x4*)(sx + 32 * mw + 16 + 4 * g) = acc[1][PN]; }
        }
    }
    if (g == q) sx[128 + 32 * mw + 16 * NH + i16] = p == 0 ? asq[0] : p == 1 ? asq[1] : p == 2 ? asq[2] : asq[3];
    __syncthreads();
    STAMP(0, 5);
    if (SWAP) {
        float mean[2], rstd[2];
        f32x4 c4[NN];
#pragma unroll
        for (int mi = 0; mi < 2; ++mi) { mean[mi] = sx[mw * 32 + mi * 16 + i16]; rstd[mi] = sx[128 + mw * 32 + mi * 16 + i16]; }
#pragma unroll
        for (int n = 0; n < NN; ++n) c4[n] = *(const f32x4*)(cs + 16 * (4 * NH + n) + 4 * g);
#pragma unroll
        for (int mi = 0; mi < 2; ++mi) {
            mean[mi] *= (1.0f / 576.0f);
            rstd[mi] = rsqrtf(fmaxf(rstd[mi] * (1.0f / 576.0f) - mean[mi] * mean[mi], 0.f) + LN_EPS);
        }
#pragma unroll
        for (int n = 0; n < NN; ++n)
#pragma unroll
            for (int pr = 0; pr < 2; ++pr)
#pragma unroll
                for (int mi = 0; mi < 2; ++mi) {
                    const float rs = MODE == 0 ? rstd[mi] * es[n][pr] : rstd[mi];
                    const unsigned pk = pk2((acc[mi][n][2 * pr] - mean[mi] * c4[n][2 * pr]) * rs,
                                            (acc[mi][n][2 * pr + 1] - mean[mi] * c4[n][2 * pr + 1]) * rs);
                    *(lds_u32)(ea[n][pr] + mi * 256) = pk;
                    if (MODE == 1) {
                        *(lds_u16)(ev[n][pr] + mi * 608) = (unsigned short)(pk & 0xffff);
                        *(lds_u16)(ev[n][pr] + mi * 608 + 16) = (unsigned short)(pk >> 16);
                    }
                }
    } else {
        f32x4 mean[2], rstd[2];
        float c1[NN];
#pragma unroll
        for (int mi = 0; mi < 2; ++mi) {
            mean[mi] = *(const f32x4*)(sx + mw * 32 + mi * 16 + 4 * g);
            rstd[mi] = *(const f32x4*)(sx + 128 + mw * 32 + mi * 16 + 4 * g);
        }
#pragma unroll
        for (int n = 0; n < NN; ++n) c1[n] = cs[16 * (4 * NH + n) + i16];
#pragma unroll
        for (int mi = 0; mi < 2; ++mi)
#pragma unroll
            for (int e = 0; e < 4; ++e) {
                mean[mi][e] *= (1.0f / 576.0f);
                rstd[mi][e] = rsqrtf(fmaxf(rstd[mi][e] * (1.0f / 576.0f) - mean[mi][e] * mean[mi][e], 0.f) + LN_EPS);
            }
#pragma unroll
        for (int n = 0; n < NN; ++n)
#pragma unroll
            for (int mi = 0; mi < 2; ++mi) {
                u32x2 o;
                o[0] = pk2((acc[mi][n][0] - mean[mi][0] * c1[n]) * rstd[mi][0], (acc[mi][n][1] - mean[mi][1] * c1[n]) * rstd[mi][1]);
                o[1] = pk2((acc[mi][n][2] - mean[mi][2] * c1[n]) * rstd[mi][2], (acc[mi][n][3] - mean[mi][3] * c1[n]) * rstd[mi][3]);
                *(lds_u32x2)(ea[n][0] + mi * 608) = o;
            }
    }
    __syncthreads();
    STAMP(0, 6);
    u32x4 cv[4];
#pragma unroll
    for (int t = 0; t < 4; ++t) cv[t] = *(const u32x4*)(smem + K1_EPI + (tid + 512 * t) * 16);
#pragma unroll
    for (int t = 0; t < 4; ++t)
        if (go[t] >= 0) st16wt(ws + go[t], cv[t]);
    STAMP(0, 7);
}

template <bool ISW>
DI void k1_loader(char* smem, const char* __restrict__ src, char* __restrict__ ws, int mt, int j, int l) {
    unsigned vo[2];
    __builtin_amdgcn_s_setprio(3);
    if (ISW) {
        const int g = l >> 4, i16 = l & 15;
#pragma unroll
        for (int t = 0; t < 2; ++t) {
            int u = (i16 >> 1) ^ (g | (t << 2)); u = u > 6 ? 6 : u;
            vo[t] = g * 3456 + u * 32 + (i16 & 1) * 16;
        }
        src += (216 * j) & ~15;
    } else {
        const int r8 = l >> 3, sl = l & 7;
#pragma unroll
        for (int t = 0; t < 2; ++t) vo[t] = r8 * 1152 + ((sl ^ ((r8 >> 1) | (t << 2))) << 4);
        src += (size_t)mt * 128 * 1152;
    }
    int kc = (2 * mt + j) % 9;
    auto issue = [&](int it) {
        char* st = smem + (it & 3) * 32768 + (ISW ? 16384 : 0);
        const char* sp = src + (ISW ? kc * 221184 : kc * 128);
#pragma unroll
        for (int i = 0; i < 16; ++i)
            glds16(sp + i * (ISW ? 13824 : 9216) + vo[ISW ? (i >> 1) & 1 : i & 1], st + i * 1024);
        kc = kc == 8 ? 0 : kc + 1;
    };
#pragma unroll 1
    for (int it = 0; it < 3; ++it) issue(it);
    asm volatile("s_waitcnt vmcnt(32)" ::: "memory");
    __builtin_amdgcn_s_barrier();
    issue(3);
#pragma unroll 1
    for (int it = 0; it < 8; ++it) {
        if (it < 6) asm volatile("s_waitcnt vmcnt(32)" ::: "memory");
        else if (it == 6) asm volatile("s_waitcnt vmcnt(16)" ::: "memory");
        else asm volatile("s_waitcnt vmcnt(0)" ::: "memory");
        __builtin_amdgcn_s_barrier();
        if (it < 5) issue(it + 4);
    }
#pragma unroll
    for (int t = 0; t < 4; ++t)
        __hip_atomic_store((gu64*)(ws + WS_GR + blockIdx.x * 4096 + ((ISW ? 4 : 0) + t) * 512 + l * 8), 0ull, RLX_AGENT);
    __builtin_amdgcn_s_barrier();
    __builtin_amdgcn_s_barrier();
}

__global__ __launch_bounds__(640) void k1_qkv(const char* __restrict__ x, const char* __restrict__ wqkv,
                                              char* __restrict__ ws) {
    __shared__ __attribute__((aligned(16))) char smem[K1_LDS];
    const int b = blockIdx.x, xcd = b & 7, i = b >> 3;
    const int mt = (xcd >> 1) * 4 + (i & 3), j = (xcd & 1) * 8 + (i >> 2);
    const int w = __builtin_amdgcn_readfirstlane(threadIdx.x >> 6), l = threadIdx.x & 63;
    if (w >= 8) {
        if (w == 8) k1_loader<false>(smem, x, ws, mt, j, l);
        else k1_loader<true>(smem, wqkv, ws, mt, j, l);
    } else if (w < 4) {
        if (j < 10) k1_body<0, 0>(smem, ws, mt, j, w, l);
        else if (j == 10) k1_body<1, 0>(smem, ws, mt, j, w, l);
        else k1_body<2, 0>(smem, ws, mt, j, w, l);
    } else {
        if (j < 10) k1_body<0, 1>(smem, ws, mt, j, w, l);
        else if (j == 10) k1_body<1, 1>(smem, ws, mt, j, w, l);
        else k1_body<2, 1>(smem, ws, mt, j, w, l);
    }
}

#define SB0 __builtin_amdgcn_sched_barrier(0)
#define EX(x) __builtin_amdgcn_exp2f(x)
#define MF(A, B, C) __builtin_amdgcn_mfma_f32_32x32x16_bf16(A, B, C, 0, 0, 0)
#define BC(x) __builtin_bit_cast(bf16x8, x)

DI f32x16 lo16(const f32x32& v) { return __builtin_shufflevector(v, v, 0, 1, 2, 3, 4, 5, 6, 7, 8, 9, 10, 11, 12, 13, 14, 15); }
DI f32x16 hi16(const f32x32& v) { return __builtin_shufflevector(v, v, 16, 17, 18, 19, 20, 21, 22, 23, 24, 25, 26, 27, 28, 29, 30, 31); }
DI bf16x8 ldsb8(const char* p) { return *(const bf16x8*)p; }

DI unsigned pkp(float a, float b) {
    unsigned r = __builtin_bit_cast(unsigned, b);
    asm("v_mov_b32_sdwa %0, %1 dst_sel:WORD_0 dst_unused:UNUSED_PRESERVE src0_sel:WORD_1" : "+v"(r) : "v"(a));
    return r;
}

struct AttnCarry { float e0, e1, e2, e3; u32x4 pc; bf16x8 v0, v1, v2, vl; };

template <int O> DI void ldv3(AttnCarry& cy, unsigned a, unsigned long long m) {
    asm volatile("s_mov_b64 exec, %4\n\tds_read_b128 %0, %3 offset:%5\n\tds_read_b128 %1, %3 offset:%6\n\t"
                 "ds_read_b128 %2, %3 offset:%7\n\ts_mov_b64 exec, -1"
                 : "+v"(cy.v0), "+v"(cy.v1), "+v"(cy.v2) : "v"(a), "s"(m), "n"(O), "n"(O + 608), "n"(O + 4864));
}
template <int O> DI void ldv1(AttnCarry& cy, unsigned a, unsigned long long m) {
    asm volatile("s_mov_b64 exec, %2\n\tds_read_b128 %0, %1 offset:%3\n\ts_mov_b64 exec, -1"
                 : "+v"(cy.vl) : "v"(a), "s"(m), "n"(O + 5472));
}

template <int PP>
DI void attn_step(const char* knext, unsigned vcur, const f32x16& sa, const f32x16& sb, f32x16& na, f32x16& nb,
                  f32x16& o, AttnCarry& cy, const bf16x8& q1, const s16x4& q2a, const s16x4& q2b, const f32x32& cinit,
                  const u32x4& k2n, unsigned long long vrow) {
    constexpr int VO = ((PP + 3) & 3) * 1216;
    s16x4 k2 = __builtin_bit_cast(s16x4, (PP & 2) ? __builtin_shufflevector(k2n, k2n, 2, 3) : __builtin_shufflevector(k2n, k2n, 0, 1));
    u32x4 pa, pb;
    float a0, a1, a2, a3, b0, b1, b2, b3;
#define EXA(s, i) a0 = EX(s[i]); a1 = EX(s[i + 1]); a2 = EX(s[i + 2]); a3 = EX(s[i + 3])
#define EXB(s, i) b0 = EX(s[i]); b1 = EX(s[i + 1]); b2 = EX(s[i + 2]); b3 = EX(s[i + 3])
    bf16x8 ka = ldsb8(knext), kb = ldsb8(knext + 4096);
    SB0;
    ldv3<VO>(cy, vcur, vrow);
    SB0;
    f32x32 n2 = __builtin_amdgcn_mfma_f32_32x32x4bf16_1k(k2, (PP & 1) ? q2b : q2a, cinit, 0, 0, 0);
    SB0;
    cy.pc[2] = pkp(cy.e0, cy.e1); cy.pc[3] = pkp(cy.e2, cy.e3);
    SB0;
    EXA(sa, 0);
    SB0;
    o = MF(cy.vl, BC(cy.pc), o);
    SB0;
    EXB(sa, 4);
    SB0;
    ldv1<VO>(cy, vcur, vrow);
    SB0;
    pa[0] = pkp(a0, a1); pa[1] = pkp(a2, a3);
    SB0;
    EXA(sa, 8);
    SB0;
    pa[2] = pkp(b0, b1); pa[3] = pkp(b2, b3);
    SB0;
    asm volatile("s_waitcnt lgkmcnt(1)");
    SB0;
    na = MF(ka, q1, lo16(n2));
    SB0;
    EXB(sa, 12);
    SB0;
    pb[0] = pkp(a0, a1); pb[1] = pkp(a2, a3);
    SB0;
    o = MF(cy.v0, BC(pa), o);
    SB0;
    EXA(sb, 0);
    SB0;
    pb[2] = pkp(b0, b1); pb[3] = pkp(b2, b3);
    SB0;
    asm volatile("s_waitcnt lgkmcnt(0)");
    SB0;
    nb = MF(kb, q1, hi16(n2));
    SB0;
    EXB(sb, 4);
    SB0;
    pa[0] = pkp(a0, a1); pa[1] = pkp(a2, a3);
    SB0;
    o = MF(cy.v1, BC(pb), o);
    SB0;
    EXA(sb, 8);
    SB0;
    pa[2] = pkp(b0, b1); pa[3] = pkp(b2, b3);
    SB0;
    cy.e0 = EX(sb[12]); cy.e1 = EX(sb[13]);
    SB0;
    o = MF(cy.v2, BC(pa), o);
    SB0;
    cy.e2 = EX(sb[14]); cy.e3 = EX(sb[15]);
    SB0;
    cy.pc[0] = pkp(a0, a1); cy.pc[1] = pkp(a2, a3);
    SB0;
#undef EXA
#undef EXB
}

template <bool FIRST>
DI f32x16 attn_pass(const char* smem, int l, const bf16x8& q1, const s16x4& q2a, const s16x4& q2b, const f32x32& cinit) {
    const int r = l & 31, h = l >> 5;
    const char* kp = smem + h * 512 + r * 16;
    unsigned vp = (unsigned)(uintptr_t)smem + 9216 + h * 304 + r * 16;
    const unsigned long long vrow = 0x0007ffff0007ffffull;
    f32x16 o = (f32x16)0.f;
    f32x16 sa, sb, ta, tb;
    AttnCarry cy;
    cy.e0 = cy.e1 = cy.e2 = cy.e3 = 0.f; cy.pc = (u32x4)0u;
    cy.v0 = cy.v1 = cy.v2 = cy.vl = BC((u32x4)0u);
    u32x4 k2 = *(const u32x4*)(kp + 8192);
    {
        s16x4 k2p = __builtin_bit_cast(s16x4, __builtin_shufflevector(k2, k2, 0, 1));
        f32x32 n2 = __builtin_amdgcn_mfma_f32_32x32x4bf16_1k(k2p, q2a, cinit, 0, 0, 0);
        sa = MF(ldsb8(kp), q1, lo16(n2));
        sb = MF(ldsb8(kp + 4096), q1, hi16(n2));
    }
#pragma unroll 1
    for (int c = 0; c < 8; ++c) {
        if (c == 4) __builtin_amdgcn_s_setprio(2);
        else if (c == 6) __builtin_amdgcn_s_setprio(1);
        else if (c == 7) __builtin_amdgcn_s_setprio(0);
        attn_step<1>(kp + 1024, vp, sa, sb, ta, tb, o, cy, q1, q2a, q2b, cinit, k2, vrow);
        attn_step<2>(kp + 2048, vp, ta, tb, sa, sb, o, cy, q1, q2a, q2b, cinit, k2, vrow);
        attn_step<3>(kp + 3072, vp, sa, sb, ta, tb, o, cy, q1, q2a, q2b, cinit, k2, vrow);
        if (FIRST && c == 0) {
            asm volatile("s_waitcnt vmcnt(0)" ::: "memory");
            __builtin_amdgcn_s_barrier();
        }
        k2 = *(const u32x4*)(kp + KCH + 8192);
        attn_step<0>(kp + KCH, vp, ta, tb, sa, sb, o, cy, q1, q2a, q2b, cinit, k2, vrow);
        kp += KCH; vp += KCH;
    }
    cy.pc[2] = pkp(cy.e0, cy.e1); cy.pc[3] = pkp(cy.e2, cy.e3);
    SB0;
    asm volatile("s_nop 1");
    SB0;
    o = MF(cy.vl, BC(cy.pc), o);
    return o;
}

__global__ __launch_bounds__(576) void k2_attn(const char* __restrict__ x, const unsigned short* __restrict__ wo, const char* __restrict__ wg,
                                               const char* __restrict__ wu, const char* __restrict__ wd, char* __restrict__ ws) {
    __shared__ __attribute__((aligned(16))) char smem[163840];
    const int b = blockIdx.x, xcd = b & 7, i = b >> 3;
    const int head = xcd * 4 + (i >> 3), qb = i & 7;
    const int w = __builtin_amdgcn_readfirstlane(threadIdx.x >> 6), l = threadIdx.x & 63;
    if (w == 8) {
        __builtin_amdgcn_s_barrier();
        __builtin_amdgcn_s_barrier();
        unsigned wv[3][8];
#pragma unroll
        for (int t = 0; t < 3; ++t) {
            const int fr = (t < 2 || b < 136) ? b + 256 * t : b;
            const int nt = fr / 18, ks = fr - nt * 18;
            const int n = nt * 16 + (l & 15), k0 = ks * 32 + (l >> 4) * 8;
#pragma unroll
            for (int e = 0; e < 8; ++e) {
                const int kk = k0 + e;
                const int row = kk < 512 ? ((kk >> 4) * 18 + (kk & 15)) : (((kk - 512) >> 1) * 18 + 16 + (kk & 1));
                asm volatile("global_load_ushort %0, %1, off" : "=v"(wv[t][e]) : "v"(wo + row * 576 + n) : "memory");
            }
        }
        char* sink = smem + KVIMG;
#pragma unroll 1
        for (int t = 0; t < 21; ++t) {
            int P = t * 32 + i; P = P < 648 ? P : 647;
            const int u = P * 64 + l;
            const int row = ((u >> 3) * 7282) >> 16;
            const unsigned o = row * 9216 + xcd * 1152 + (u - row * 72) * 16;
            glds16(wg + o, sink);
            glds16(wu + o, sink + 1024);
            glds16(wd + xcd * 663552 + u * 16, sink + 2048);
            asm volatile("s_waitcnt vmcnt(48)" ::: "memory");
        }
#pragma unroll 1
        for (int t = 0; t < 9; ++t) glds16(x + xcd * 294912 + (t * 32 + i) * 1024 + l * 16, sink + 3072);
        asm volatile("s_waitcnt vmcnt(0)" ::: "memory");
        SB0;
#pragma unroll
        for (int t = 0; t < 3; ++t) {
            u32x4 v;
#pragma unroll
            for (int e = 0; e < 4; ++e) {
                asm volatile("" : "+v"(wv[t][2 * e]), "+v"(wv[t][2 * e + 1]));
                v[e] = wv[t][2 * e] | (wv[t][2 * e + 1] << 16);
            }
            if (t < 2 || b < 136) st16wt(ws + WS_WO + (size_t)(b + 256 * t) * 1024 + l * 16, v);
        }
        return;
    }
    const int r = l & 31, h = l >> 5;
    const int qt = qb * 8 + w;

    STAMP(1, 0);
    __builtin_amdgcn_s_setprio(3);
    const char* qp = ws + WS_Q + (size_t)head * QIMG + (size_t)qb * QCH;
    bf16x8 q1; unsigned q2;
    asm volatile("global_load_dwordx4 %0, %1, off" : "=v"(q1) : "v"(qp + w * 1024 + h * 512 + r * 16) : "memory");
    asm volatile("global_load_dword %0, %1, off" : "=v"(q2) : "v"(qp + 8192 + (w >> 2) * 512 + r * 16 + (w & 3) * 4) : "memory");
    SB0;
    {
        const char* src = ws + WS_KV + (size_t)head * KVIMG + w * 1024 + l * 16;
        char* dst = smem + w * 1024;
#pragma unroll
        for (int t = 0; t < 19; ++t) glds16(src + t * 8192, dst + t * 8192);
    }
    asm volatile("s_waitcnt vmcnt(16)" ::: "memory");
    SB0;
    asm volatile("" : "+v"(q1), "+v"(q2));
    u32x2 qa; qa[0] = q2; qa[1] = 0u;
    u32x2 qbv; qbv[0] = 0u; qbv[1] = q2;
    const s16x4 q2a = __builtin_bit_cast(s16x4, qa), q2b = __builtin_bit_cast(s16x4, qbv);
    f32x32 cinit = (f32x32)0.f;
    __builtin_amdgcn_s_barrier();
    SB0;
    STAMP(1, 2);
    STAMPC(1, 14);
    f32x16 o = attn_pass<true>(smem, l, q1, q2a, q2b, cinit);
    STAMP(1, 3);
    STAMPC(1, 15);
    auto denom = [&]() {
        const float o10 = o[10];
        const unsigned lb = __builtin_bit_cast(unsigned, o10);
        auto sw = __builtin_amdgcn_permlane32_swap(lb, lb, false, false);
        const unsigned l0 = sw[0];
        return __builtin_bit_cast(float, l0);
    };
    float lsum = denom();
    bool ok = (lsum >= 9.0e-13f) && (lsum <= 1.0e18f);
#pragma unroll
    for (int e = 0; e < 8; ++e) ok = ok && (fabsf(o[e]) <= 3.0e38f);
    ok = ok && (h == 1 || ((fabsf(o[8]) <= 3.0e38f) && (fabsf(o[9]) <= 3.0e38f)));
    if (!__all(ok)) {
        const char* kp = smem + h * 512 + r * 16;
        float m = -3.0e38f;
#pragma unroll 1
        for (int c = 0; c < 8; ++c) {
            u32x4 k2 = *(const u32x4*)(kp + 8192);
#pragma unroll
            for (int pp = 0; pp < 4; ++pp) {
                s16x4 k2p = __builtin_bit_cast(s16x4, (pp & 2) ? __builtin_shufflevector(k2, k2, 2, 3) : __builtin_shufflevector(k2, k2, 0, 1));
                f32x32 n2 = __builtin_amdgcn_mfma_f32_32x32x4bf16_1k(k2p, (pp & 1) ? q2b : q2a, cinit, 0, 0, 0);
                f32x16 sa = MF(ldsb8(kp + pp * 1024), q1, lo16(n2));
                f32x16 sb = MF(ldsb8(kp + pp * 1024 + 4096), q1, hi16(n2));
#pragma unroll
                for (int e = 0; e < 16; ++e) m = fmaxf(m, fmaxf(sa[e], sb[e]));
            }
            kp += KCH;
        }
        m = fmaxf(m, __shfl_xor(m, 32));
        cinit = (f32x32)(-m);
        o = attn_pass<false>(smem, l, q1, q2a, q2b, cinit);
        lsum = denom();
    }
    const float inv = 1.0f / lsum;
    char* op = ws + WS_O + ((size_t)(qt * 2 + (r >> 4)) * 18) * 1024;
    {
        u32x2 a, c;
        a[0] = pk2(o[0] * inv, o[1] * inv); a[1] = pk2(o[2] * inv, o[3] * inv);
        c[0] = pk2(o[4] * inv, o[5] * inv); c[1] = pk2(o[6] * inv, o[7] * inv);
        char* mp = op + (head >> 1) * 1024 + (head & 1) * 512 + (r & 15) * 16 + h * 8;
        *(u32x2*)mp = a;
        *(u32x2*)(mp + 256) = c;
        if (h == 0)
            *(unsigned*)(op + (16 + (head >> 4)) * 1024 + ((head >> 2) & 3) * 256 + (r & 15) * 16 + (head & 3) * 4) =
                pk2(o[8] * inv, o[9] * inv);
    }
    STAMP(1, 4);
}

__global__ __launch_bounds__(576, 1) void k3_oproj(const char* __restrict__ x, const char* __restrict__ wr,
                                                   char* __restrict__ ws, char* __restrict__ out) {
    __shared__ __attribute__((aligned(16))) char smem[36864 + 9728 + 9 * 96 * 4 + 96 * 4 + 64];
    const int b = blockIdx.x, xcd = b & 7, i = b >> 3;
    const int mb = xcd * 8 + (i >> 2), ns = i & 3;
    const int tid = threadIdx.x;
    const int w = __builtin_amdgcn_readfirstlane(tid >> 6), l = tid & 63;
    const int g = l >> 4, i16 = l & 15;
    const int nt = ns * 9 + w;
    STAMP(2, 0);
    const char* osrc = ws + WS_O + (size_t)mb * 36864 + w * 1024 + l * 16;
    u32x4 ot[4];
#pragma unroll
    for (int t = 0; t < 4; ++t) ot[t] = *(const u32x4*)(osrc + t * 9216);
    SB0;
    STAMP(2, 6);
    __builtin_amdgcn_s_barrier();
    SB0;
    STAMP(2, 7);
    const int c0 = nt * 16 + 4 * g;
    u32x2 xr[2];
#pragma unroll
    for (int mi = 0; mi < 2; ++mi) xr[mi] = *(const u32x2*)(x + (size_t)(mb * 32 + mi * 16 + i16) * 1152 + c0 * 2);
    const u32x2 wrv = *(const u32x2*)(wr + c0 * 2);
    float wsum = bf2f(((const unsigned short*)wr)[tid]);
    const char* wsrc = ws + WS_WO + (size_t)nt * 18432 + l * 16;
    bf16x8 wf[18];
#pragma unroll
    for (int ks = 0; ks < 5; ++ks) wf[ks] = *(const bf16x8*)(wsrc + ks * 1024);
    SB0;
    STAMP(2, 8);
#pragma unroll
    for (int t = 0; t < 4; ++t) *(u32x4*)(smem + w * 1024 + l * 16 + t * 9216) = ot[t];
    SB0;
#pragma unroll
    for (int ks = 5; ks < 9; ++ks) wf[ks] = *(const bf16x8*)(wsrc + ks * 1024);
    asm volatile("s_waitcnt lgkmcnt(0)" ::: "memory");
    STAMP(2, 9);
    __builtin_amdgcn_s_barrier();
    asm volatile("" ::: "memory");
    SB0;
    STAMP(2, 1);
    f32x4 acc[2] = {(f32x4)0.f, (f32x4)0.f};
#pragma unroll
    for (int ks = 0; ks < 18; ++ks) {
        if (ks + 9 < 18) wf[ks + 9] = *(const bf16x8*)(wsrc + (ks + 9) * 1024);
#pragma unroll
        for (int mi = 0; mi < 2; ++mi) {
            bf16x8 of = *(const bf16x8*)(smem + (mi * 18 + ks) * 1024 + l * 16);
            acc[mi] = __builtin_amdgcn_mfma_f32_16x16x32_bf16(wf[ks], of, acc[mi], 0, 0, 0);
        }
        if ((ks & 1) == 1) SB0;
    }
    STAMP(2, 2);
    char* ht = smem + 36864;
    float* red = (float*)(smem + 36864 + 9728);
    float* tot = red + 9 * 96;
    float* wsm = tot + 96;
    const float w0 = bflo(wrv[0]), w1 = bfhi(wrv[0]), w2 = bflo(wrv[1]), w3 = bfhi(wrv[1]);
    float s[2], qq[2], e[2];
#pragma unroll
    for (int mi = 0; mi < 2; ++mi) {
        u32x2 hv;
        hv[0] = pk2(bflo(xr[mi][0]) + rbf(acc[mi][0]), bfhi(xr[mi][0]) + rbf(acc[mi][1]));
        hv[1] = pk2(bflo(xr[mi][1]) + rbf(acc[mi][2]), bfhi(xr[mi][1]) + rbf(acc[mi][3]));
        *(u32x2*)(ht + (mi * 16 + i16) * 304 + w * 32 + g * 8) = hv;
        const float h0 = bflo(hv[0]), h1 = bfhi(hv[0]), h2 = bflo(hv[1]), h3 = bfhi(hv[1]);
        s[mi] = (h0 + h1) + (h2 + h3);
        qq[mi] = (h0 * h0 + h1 * h1) + (h2 * h2 + h3 * h3);
        e[mi] = (h0 * w0 + h1 * w1) + (h2 * w2 + h3 * w3);
    }
    {
        const float s1 = sw16(s[0], s[1]), q1 = sw16(qq[0], qq[1]), e1 = sw16(e[0], e[1]);
        red[w * 96 + l] = sw32(s1, q1);
        const float e2 = sw32(e1, e1);
        if (l < 32) red[w * 96 + 64 + l] = e2;
        wsum += rorf<1>(wsum); wsum += rorf<2>(wsum); wsum += rorf<4>(wsum); wsum += rorf<8>(wsum);
        wsum = sw16(wsum, wsum); wsum = sw32(wsum, wsum);
        if (l == 0) wsm[w] = wsum;
    }
    __syncthreads();
    STAMP(2, 3);
    if (tid < 96) {
        float a = 0.f;
#pragma unroll
        for (int ww = 0; ww < 9; ++ww) a += red[ww * 96 + tid];
        char* gr = ws + WS_GR3 + (size_t)mb * 3072 + tid * 8;
        put_granule(gr + ns * 768, a);
        float v[4];
        get_granules<4>(gr, 768, v);
        tot[tid] = (v[0] + v[1]) + (v[2] + v[3]);
    }
    const int row = tid / 18, ch = tid - row * 18;
    const u32x4 hv = *(const u32x4*)(ht + row * 304 + ch * 16);
    const size_t go = (size_t)(mb * 32 + row) * 1152 + ns * 288 + ch * 16;
    *(u32x4*)(ws + WS_H + go) = hv;
    __syncthreads();
    STAMP(2, 4);
    {
        const float mean = tot[row] * (1.0f / 576.0f);
        const float rstd = rsqrtf(fmaxf(tot[32 + row] * (1.0f / 576.0f) - mean * mean, 0.f) + LN_EPS);
        u32x4 ov;
#pragma unroll
        for (int k = 0; k < 4; ++k) {
            const float a = bflo(hv[k]), c = bfhi(hv[k]);
            ov[k] = pk2(a + rbf((a - mean) * rstd), c + rbf((c - mean) * rstd));
        }
        *(u32x4*)(out + go) = ov;
    }
    if (ns == 0 && w == 8) {
        float wt = 0.f;
#pragma unroll
        for (int ww = 0; ww < 9; ++ww) wt += wsm[ww];
        const int r = l & 31;
        const float mean = tot[r] * (1.0f / 576.0f);
        const float rstd = rsqrtf(fmaxf(tot[32 + r] * (1.0f / 576.0f) - mean * mean, 0.f) + LN_EPS);
        const float rv = (tot[64 + r] - mean * wt) * rstd;
        if (l == argmax_lane(rv))
            *(f32x4*)(ws + WS_CAND + mb * 16) = (f32x4){rv, __builtin_bit_cast(float, mb * 32 + r), mean, rstd};
    }
    STAMP(2, 5);
}

#define K4A 1
__global__ __launch_bounds__(576, 1) void k4_ffn(const char* __restrict__ wg, const char* __restrict__ wu,
                                                 const char* __restrict__ wd, char* __restrict__ ws,
                                                 char* __restrict__ out) {
    __shared__ __attribute__((aligned(16))) float sm[9 * 64 + 9 * 16];
    const int tid = threadIdx.x;
    const int bx = blockIdx.x;
    const int f = (bx & 7) * 18 + (bx >> 3);
    const int w = __builtin_amdgcn_readfirstlane(tid >> 6), l = tid & 63;
    STAMP(3, 0);
    const f32x4 cand = *(const f32x4*)(ws + WS_CAND + l * 16);
    SB0;
    __builtin_amdgcn_s_barrier();
    SB0;
    const int kr = tid >> 2, cq = tid & 3;
    u32x4 gv[4], uv[4], dv[4];
#pragma unroll
    for (int t = 0; t < K4A; ++t) {
        gv[t] = *(const u32x4*)(wg + (size_t)(kr + 144 * t) * (F_ * 2) + f * 64 + cq * 16);
        uv[t] = *(const u32x4*)(wu + (size_t)(kr + 144 * t) * (F_ * 2) + f * 64 + cq * 16);
    }
    const int rg = l >> 3;
    SB0;
    const float c0 = cand[0], c1 = cand[1], c2 = cand[2], c3 = cand[3];
    const int bl = argmax_lane(c0);
    const float best = __builtin_bit_cast(float, __builtin_amdgcn_readlane(__builtin_bit_cast(int, c0), bl));
    const int bidx = __builtin_amdgcn_readlane(__builtin_bit_cast(int, c1), bl) & (S_ - 1);
    const float bmean = __builtin_bit_cast(float, __builtin_amdgcn_readlane(__builtin_bit_cast(int, c2), bl));
    const float brstd = __builtin_bit_cast(float, __builtin_amdgcn_readlane(__builtin_bit_cast(int, c3), bl));
    STAMP(3, 1);
    const unsigned short* hrow = (const unsigned short*)(ws + WS_H + (size_t)bidx * 1152);
    unsigned short hr[4];
#pragma unroll
    for (int t = 0; t < 4; ++t) hr[t] = hrow[kr + 144 * t];
    const unsigned short hfin = hrow[(bx < 36 ? bx : 0) * 16 + (l & 15)];
    SB0;
#pragma unroll
    for (int t = K4A; t < 4; ++t) {
        gv[t] = *(const u32x4*)(wg + (size_t)(kr + 144 * t) * (F_ * 2) + f * 64 + cq * 16);
        uv[t] = *(const u32x4*)(wu + (size_t)(kr + 144 * t) * (F_ * 2) + f * 64 + cq * 16);
    }
#pragma unroll
    for (int t = 0; t < 4; ++t) dv[t] = *(const u32x4*)(wd + (size_t)(f * 32 + rg + 8 * t) * 1152 + w * 128 + (l & 7) * 16);
    SB0;
    float ga[8], ua[8];
#pragma unroll
    for (int e = 0; e < 8; ++e) { ga[e] = 0.f; ua[e] = 0.f; }
#pragma unroll
    for (int t = 0; t < 4; ++t) {
        const float xs = rbf((bf2f(hr[t]) - bmean) * brstd);
#pragma unroll
        for (int e = 0; e < 4; ++e) {
            ga[2 * e] += xs * bflo(gv[t][e]); ga[2 * e + 1] += xs * bfhi(gv[t][e]);
            ua[2 * e] += xs * bflo(uv[t][e]); ua[2 * e + 1] += xs * bfhi(uv[t][e]);
        }
    }
    STAMP(3, 2);
    {
        float v[8]; f32x4 z;
#pragma unroll
        for (int e = 0; e < 8; ++e) v[e] = sw32(ga[e], ua[e]);
#pragma unroll
        for (int e = 0; e < 4; ++e) { float t = sw16(v[e], v[e + 4]); t += rorf<8>(t); t += rorf<4>(t); z[e] = t; }
        if ((l & 12) == 0) *(f32x4*)(sm + w * 64 + (l >> 5) * 32 + cq * 8 + ((l >> 4) & 1) * 4) = z;
    }
    asm volatile("s_waitcnt lgkmcnt(0)" ::: "memory");
    __builtin_amdgcn_s_barrier();
    asm volatile("" ::: "memory");
    STAMP(3, 3);
    float act;
    {
        float a = 0.f;
#pragma unroll
        for (int p = 0; p < 9; ++p) a += sm[p * 64 + l];
        const unsigned ab = __builtin_bit_cast(unsigned, rbf(a));
        auto r = __builtin_amdgcn_permlane32_swap(ab, ab, false, false);
        const unsigned r0 = r[0], r1 = r[1];
        const float gt = __builtin_bit_cast(float, r0), up = __builtin_bit_cast(float, r1);
        act = rbf(rbf(gt / (1.0f + __expf(-gt))) * up);
    }
    float ya[8];
#pragma unroll
    for (int e = 0; e < 8; ++e) ya[e] = 0.f;
#pragma unroll
    for (int t = 0; t < 4; ++t) {
        const float a = __shfl(act, rg + 8 * t);
#pragma unroll
        for (int e = 0; e < 4; ++e) { ya[2 * e] += a * bflo(dv[t][e]); ya[2 * e + 1] += a * bfhi(dv[t][e]); }
    }
    {
        float z[4];
#pragma unroll
        for (int e = 0; e < 4; ++e) z[e] = sw32(ya[e], ya[e + 4]);
        float u0 = sw16(z[0], z[2]), u1 = sw16(z[1], z[3]);
        u0 += rorf<8>(u0); u1 += rorf<8>(u1);
        put_granule(ws + WS_GR4 + ((size_t)f * 576 + w * 64 + (l & 7) * 8 + rg) * 8, (l & 8) ? u1 : u0);
    }
    STAMP(3, 4);
    if (bx < 36) {
        float v[4];
        get_granules<4>(ws + WS_GR4 + ((size_t)(tid >> 4) * 576 + bx * 16 + (l & 15)) * 8, 36 * 576 * 8, v);
        float a = (v[0] + v[1]) + (v[2] + v[3]);
        a = sw16(a, a); a = sw32(a, a);
        if (l < 16) sm[576 + w * 16 + l] = a;
        __syncthreads();
        if (tid < 16) {
            float y = 0.f;
#pragma unroll
            for (int t = 0; t < 9; ++t) y += sm[576 + t * 16 + tid];
            const float hv = bf2f(hfin);
            const float sg = rbf(1.0f / (1.0f + __expf(-rbf(best))));
            const float hn = rbf((hv - bmean) * brstd);
            const float upd = rbf(rbf(y) * sg);
            __bf16 o = (__bf16)(hv + rbf(hn + upd));
            ((unsigned short*)out)[(size_t)bidx * 576 + bx * 16 + tid] = __builtin_bit_cast(unsigned short, o);
        }
        STAMP(3, 5);
    }
}

extern "C" void kernel_launch(void* const* d_in, const int* in_sizes, int n_in,
                              void* d_out, int out_size, void* d_ws, size_t ws_size,
                              hipStream_t stream) {
    const char* x = (const char*)d_in[0];
    const char* wqkv = (const char*)d_in[1];
    const unsigned short* wo = (const unsigned short*)d_in[2];
    const char* wr = (const char*)d_in[4];
    const char* wg = (const char*)d_in[5];
    const char* wu = (const char*)d_in[6];
    const char* wd = (const char*)d_in[7];
    char* ws = (char*)d_ws;
    char* out = (char*)d_out;
    k1_qkv<<<256, 640, 0, stream>>>(x, wqkv, ws);
    k2_attn<<<256, 576, 0, stream>>>(x, wo, wg, wu, wd, ws);
    k3_oproj<<<256, 576, 0, stream>>>(x, wr, ws, out);
    k4_ffn<<<144, 576, 0, stream>>>(wg, wu, wd, ws, out);
}
```

```cpp
#include <hip/hip_runtime.h>
#include <hip/hip_bf16.h>
#include <stdint.h>

typedef __attribute__((ext_vector_type(4))) float f32x4;
typedef __attribute__((ext_vector_type(16))) float f32x16;
typedef __attribute__((ext_vector_type(32))) float f32x32;
typedef __attribute__((ext_vector_type(8))) __bf16 bf16x8;
typedef __attribute__((ext_vector_type(2))) __bf16 bf16x2;
typedef __attribute__((ext_vector_type(4))) short s16x4;
typedef __attribute__((ext_vector_type(8))) short s16x8;
typedef __attribute__((ext_vector_type(4))) unsigned u32x4;
typedef __attribute__((ext_vector_type(2))) unsigned u32x2;
typedef __attribute__((address_space(3))) s16x4* lds_s16x4_ptr;

#define DI __device__ __forceinline__

#define STAMP(kid, i)
#define STAMPC(kid, i)

constexpr int S_ = 2048, D_ = 576, F_ = 4608;
constexpr float LN_EPS = 1e-5f;

constexpr int QCH = 9216;
constexpr int KCH = 18944;
constexpr int QIMG = 8 * QCH;
constexpr int KVIMG = 155648;
constexpr size_t WS_Q = 0;
constexpr size_t WS_KV = WS_Q + (size_t)32 * QIMG;
constexpr size_t WS_O = WS_KV + (size_t)32 * KVIMG;
constexpr size_t WS_WO = WS_O + (size_t)128 * 18 * 1024;
constexpr size_t WS_GR = WS_WO + (size_t)36 * 18 * 1024;
constexpr size_t WS_GR3 = WS_GR;
constexpr size_t WS_GR4 = WS_GR + (size_t)256 * 768;
constexpr size_t WS_ROWG = WS_GR4 + (size_t)144 * 576 * 8;
constexpr size_t WS_CANDG = WS_ROWG + (size_t)64 * 288 * 8;
constexpr size_t WS_CNT = WS_CANDG + 2048;
static_assert(WS_CNT + 64 * 128 <= WS_GR + (1u << 20), "hand-off area");

typedef __attribute__((address_space(1))) unsigned gu32;
typedef __attribute__((address_space(1))) unsigned long long gu64;
#define RLX_AGENT __ATOMIC_RELAXED, __HIP_MEMORY_SCOPE_AGENT
constexpr unsigned SPIN_CAP = 1u << 20;
DI void put_granule_u(char* p, unsigned v) { __hip_atomic_store((gu64*)p, (1ull << 32) | v, RLX_AGENT); }
DI void put_granule(char* p, float v) { put_granule_u(p, __builtin_bit_cast(unsigned, v)); }
template <int N>
DI void get_granules(const char* p, int stride, float (&v)[N]) {
    for (unsigned spins = 0; spins < SPIN_CAP; ++spins) {
        bool ok = true;
#pragma unroll
        for (int k = 0; k < N; ++k) {
            const unsigned long long x = __hip_atomic_load((gu64*)(p + k * stride), RLX_AGENT);
            v[k] = __builtin_bit_cast(float, (unsigned)x); ok &= (x >> 32) == 1ull;
        }
        if (__all(ok)) break;
        __builtin_amdgcn_s_sleep(1);
    }
}
DI float sw32(float a, float b) {
    auto r = __builtin_amdgcn_permlane32_swap(__builtin_bit_cast(unsigned, a), __builtin_bit_cast(unsigned, b), false, false);
    const unsigned r0 = r[0], r1 = r[1];
    return __builtin_bit_cast(float, r0) + __builtin_bit_cast(float, r1);
}
DI float sw16(float a, float b) {
    auto r = __builtin_amdgcn_permlane16_swap(__builtin_bit_cast(unsigned, a), __builtin_bit_cast(unsigned, b), false, false);
    const unsigned r0 = r[0], r1 = r[1];
    return __builtin_bit_cast(float, r0) + __builtin_bit_cast(float, r1);
}
template <int R> DI unsigned ror(unsigned v) { return (unsigned)__builtin_amdgcn_update_dpp(0, (int)v, 0x120 + R, 0xf, 0xf, false); }
template <int R> DI float rorf(float v) { return __builtin_bit_cast(float, ror<R>(__builtin_bit_cast(unsigned, v))); }
DI int argmax_lane(float v) {
    const unsigned u = __builtin_bit_cast(unsigned, v);
    const unsigned key = (u & 0x80000000u) ? ~u : (u | 0x80000000u);
    unsigned m = key;
    m = max(m, ror<1>(m)); m = max(m, ror<2>(m)); m = max(m, ror<4>(m)); m = max(m, ror<8>(m));
    { auto r = __builtin_amdgcn_permlane16_swap(m, m, false, false); const unsigned r0 = r[0], r1 = r[1]; m = max(r0, r1); }
    { auto r = __builtin_amdgcn_permlane32_swap(m, m, false, false); const unsigned r0 = r[0], r1 = r[1]; m = max(r0, r1); }
    return __builtin_ctzll(__ballot(key == m));
}

DI float bf2f(unsigned short v) { return __builtin_bit_cast(float, (unsigned)v << 16); }
DI float bflo(unsigned v) { return __builtin_bit_cast(float, v << 16); }
DI float bfhi(unsigned v) { return __builtin_bit_cast(float, v & 0xffff0000u); }
DI unsigned pk2(float a, float b) {
    bf16x2 r; r[0] = (__bf16)a; r[1] = (__bf16)b;
    return __builtin_bit_cast(unsigned, r);
}
DI float rbf(float a) { return (float)(__bf16)a; }

#define WT " sc1"
DI void st16wt(void* p, const u32x4& v) { asm volatile("global_store_dwordx4 %0, %1, off" WT "\n\ts_nop 2" :: "v"(p), "v"(v) : "memory"); }
DI void st8wt(void* p, const u32x2& v) { asm volatile("global_store_dwordx2 %0, %1, off" WT "\n\ts_nop 2" :: "v"(p), "v"(v) : "memory"); }
DI void st4wt(void* p, unsigned v) { asm volatile("global_store_dword %0, %1, off" WT :: "v"(p), "v"(v) : "memory"); }

DI void glds16(const void* g, void* lds) {
    __builtin_amdgcn_global_load_lds((const __attribute__((address_space(1))) unsigned*)g,
                                     (__attribute__((address_space(3))) unsigned*)lds, 16, 0, 0);
}

constexpr int K1_EPI = 131072;
constexpr int K1_CS = K1_EPI + 6 * 4864;
constexpr int K1_ST = K1_CS + 512;
constexpr int K1_DUM = K1_ST + 1024;
constexpr int K1_LDS = 163840;

struct K1Frag { bf16x8 x[2][2]; bf16x8 w[2][5]; };
typedef __attribute__((address_space(3))) unsigned* lds_u32;
typedef __attribute__((address_space(3))) unsigned short* lds_u16;
typedef __attribute__((address_space(3))) u32x2* lds_u32x2;

template <int MODE, int NH>
DI void k1_body(char* smem, char* __restrict__ ws, int mt, int j, int w, int l) {
    constexpr bool SWAP = MODE != 2;
    constexpr int NW = NH ? 5 : 4, NN = NH ? 3 : 4;
    const int g = l >> 4, i16 = l & 15;
    const int mw = w & 3;
    STAMP(0, 0);
    f32x4 acc[2][4], asq = (f32x4)0.f;
#pragma unroll
    for (int n = 0; n < 4; ++n) { acc[0][n] = (f32x4)0.f; acc[1][n] = (f32x4)0.f; }
    bf16x8 ones;
#pragma unroll
    for (int e = 0; e < 8; ++e) ones[e] = (__bf16)1.0f;

    const int q = i16 >> 2, p = i16 & 3;
    const int fl = q | ((g & 1) << 2);
    const unsigned sbase = (unsigned)(uintptr_t)smem;
    unsigned xa[2];
    xa[0] = sbase + mw * 4096 + i16 * 128 + ((g ^ (i16 >> 1)) << 4); xa[1] = xa[0] ^ 64;
    const unsigned wrow = sbase + 16384 + (8 * g + q) * 256 + p * 8;
    unsigned wa[NW];
#pragma unroll
    for (int n = 0; n < NN; ++n) wa[n] = wrow + (((4 * NH + n) ^ fl) << 5);
    if (NH) { wa[3] = wrow + (((2 * mw) ^ fl) << 5); wa[NW - 1] = wrow + (((2 * mw + 1) ^ fl) << 5); }
    const int odd = j & 1, off = odd * 4;
    const bool patch = NH ? (!odd && i16 >= 12) : (odd && i16 < 4);

    const int tid = w * 64 + l;
    unsigned ea[NN][2], ev[NN][2]; float es[NN][2];
    const unsigned epi = sbase + K1_EPI, dum = sbase + K1_DUM + l * 8;
#pragma unroll
    for (int n = 0; n < NN; ++n) {
#pragma unroll
        for (int pr = 0; pr < (SWAP ? 2 : 1); ++pr) {
            const int col = SWAP ? 16 * (4 * NH + n) + 4 * g + 2 * pr - off : 16 * (4 * NH + n) + i16 - off;
            const bool valid = (col >= 0) && (col < 108);
            const int sI = (col * 57) >> 10;
            const int d = col - 18 * sI;
            const int gs = 6 * j + sI;
            if (SWAP) {
                const unsigned qk = epi + sI * 4864 + i16 * 16 + (d < 16 ? mw * 1024 + (d >> 3) * 512 + (d & 7) * 2 : 4096 + mw * 4);
                ea[n][pr] = (valid && gs < 64) ? qk : dum;
                asm volatile("" : "+v"(ea[n][pr]));
                if (MODE == 0) {
                    es[n][pr] = gs < 32 ? 0.23570226039551584f * 1.4426950408889634f : 1.0f;
                    asm volatile("" : "+v"(es[n][pr]));
                } else {
                    const unsigned vv = epi + sI * 4864 + mw * 1216 + ((i16 >> 2) & 1) * 304 + d * 16 + (((i16 >> 3) << 2) | (i16 & 3)) * 2;
                    ev[n][pr] = (valid && gs >= 64) ? vv : dum;
                    asm volatile("" : "+v"(ev[n][pr]));
                }
            } else {
                const unsigned vv = epi + sI * 4864 + mw * 1216 + (g & 1) * 304 + d * 16 + (g >> 1) * 8;
                ea[n][0] = valid ? vv : dum;
                asm volatile("" : "+v"(ea[n][0]));
            }
        }
    }
    int go[4];
    const int ch = mt >> 1, hb = mt & 1;
#pragma unroll
    for (int t = 0; t < 4; ++t) {
        const int un = tid + 512 * t;
        const int sI = (un * 3450) >> 20;
        const int o = (un - sI * 304) * 16;
        const int gs = 6 * j + sI, part = gs >> 5, head = gs & 31;
        const int base = part == 0 ? (int)WS_Q + head * QIMG + ch * QCH : (int)WS_KV + head * KVIMG + ch * KCH;
        const int dst = part == 2 ? 9216 + hb * 4864 + o : (o < 4096 ? hb * 4096 + o : 4096 + hb * 512 + o);
        go[t] = (un < 6 * 304 && (part == 2 || o < 4608)) ? base + dst : -1;
        asm volatile("" : "+v"(go[t]));
    }
    if (MODE != 0 && tid < 96 && 6 * j + (tid >> 4) >= 64)
        *(u32x4*)(smem + K1_EPI + (tid >> 4) * 4864 + ((tid >> 1) & 7) * 608 + (tid & 1) * 304 + 288) = (u32x4)0x3f803f80u;

    typedef __attribute__((address_space(3))) const bf16x8* lds_b8;
    auto rd = [&](K1Frag& f, unsigned so, int i) {
        const int kk = i / (NW + 1), t = i % (NW + 1);
        if (t == 0) {
            f.x[kk][0] = *(lds_b8)(xa[kk] + so);
            f.x[kk][1] = *(lds_b8)(xa[kk] + so + 2048);
        } else {
            s16x4 lo = __builtin_amdgcn_ds_read_tr16_b64_v4i16((lds_s16x4_ptr)(wa[t - 1] + so + kk * 8192));
            s16x4 hi = __builtin_amdgcn_ds_read_tr16_b64_v4i16((lds_s16x4_ptr)(wa[t - 1] + so + kk * 8192 + 1024));
            f.w[kk][t - 1] = __builtin_bit_cast(bf16x8, __builtin_shufflevector(lo, hi, 0, 1, 2, 3, 4, 5, 6, 7));
        }
    };
    auto mm = [&](const bf16x8& wf, const bf16x8& xf, f32x4& c) {
        c = SWAP ? __builtin_amdgcn_mfma_f32_16x16x32_bf16(wf, xf, c, 0, 0, 0)
                 : __builtin_amdgcn_mfma_f32_16x16x32_bf16(xf, wf, c, 0, 0, 0);
    };
    auto compute = [&](const K1Frag& cur, K1Frag& nxt, unsigned so, bool pf) {
        constexpr int PN = NH ? 2 : 0;
        bf16x8 wp[2];
#pragma unroll
        for (int m = 0; m < 18; ++m) {
            const int kk = m / 9, t = m % 9;
            if (t == 0) mm(cur.x[kk][NH], cur.x[kk][NH], asq);
            else if (t >= 7) mm(wp[kk], cur.x[kk][t - 7], acc[t - 7][PN]);
            else if (NH == 0) mm(cur.w[kk][(t + 1) >> 1], cur.x[kk][(t + 1) & 1], acc[(t + 1) & 1][(t + 1) >> 1]);
            else if (t <= 2) mm(cur.w[kk][t + 2], ones, acc[t - 1][3]);
            else mm(cur.w[kk][(t - 3) >> 1], cur.x[kk][(t - 3) & 1], acc[(t - 3) & 1][(t - 3) >> 1]);
            __builtin_amdgcn_sched_barrier(0);
            if (pf && m < 2 * (NW + 1)) rd(nxt, so, m);
            if (m >= 12 - 12 * (1 - kk) + 2 && m < 12 - 12 * (1 - kk) + 3) {
                u32x4 v = __builtin_bit_cast(u32x4, cur.w[kk][PN]);
#pragma unroll
                for (int e = 0; e < 4; ++e) v[e] = patch ? 0x3f803f80u : v[e];
                wp[kk] = __builtin_bit_cast(bf16x8, v);
            }
            __builtin_amdgcn_sched_barrier(0);
        }
    };
    K1Frag fa, fb;
    __builtin_amdgcn_s_barrier();
    __builtin_amdgcn_sched_barrier(0);
    STAMP(0, 2);
    STAMPC(0, 14);
#pragma unroll
    for (int i = 0; i < 2 * (NW + 1); ++i) rd(fa, 0, i);
#pragma unroll 1
    for (int it = 0; it < 8; it += 2) {
        asm volatile("s_waitcnt lgkmcnt(0)" ::: "memory");
        __builtin_amdgcn_s_barrier();
        __builtin_amdgcn_sched_barrier(0);
        compute(fa, fb, 32768, true);
        xa[0] ^= 65536; xa[1] ^= 65536;
#pragma unroll
        for (int n = 0; n < NW; ++n) wa[n] ^= 65536;
        asm volatile("s_waitcnt lgkmcnt(0)" ::: "memory");
        __builtin_amdgcn_s_barrier();
        __builtin_amdgcn_sched_barrier(0);
        compute(fb, fa, 0, true);
    }
    compute(fa, fb, 0, false);
    STAMPC(0, 15);
    STAMP(0, 3);
    STAMP(0, 4);

    float* cs = (float*)(smem + K1_CS);
    float* sx = (float*)(smem + K1_ST);
    if (NH == 1) {
        if (SWAP) {
            if (i16 == 0) { *(f32x4*)(cs + 32 * mw + 4 * g) = acc[0][3]; *(f32x4*)(cs + 32 * mw + 16 + 4 * g) = acc[1][3]; }
        } else {
            if (g == 0) { cs[32 * mw + i16] = acc[0][3][0]; cs[32 * mw + 16 + i16] = acc[1][3][0]; }
        }
    }
    if (odd != NH) {
        constexpr int PN = NH ? 2 : 0;
        if (SWAP) {
            if (g == (NH ? 3 : 0)) { sx[32 * mw + i16] = acc[0][PN][0]; sx[32 * mw + 16 + i16] = acc[1][PN][0]; }
        } else {
            if (i16 == (NH ? 12 : 0)) { *(f32x4*)(sx + 32 * mw + 4 * g) = acc[0][PN]; *(f32x4*)(sx + 32 * mw + 16 + 4 * g) = acc[1][PN]; }
        }
    }
    if (g == q) sx[128 + 32 * mw + 16 * NH + i16] = p == 0 ? asq[0] : p == 1 ? asq[1] : p == 2 ? asq[2] : asq[3];
    __syncthreads();
    STAMP(0, 5);
    if (SWAP) {
        float mean[2], rstd[2];
        f32x4 c4[NN];
#pragma unroll
        for (int mi = 0; mi < 2; ++mi) { mean[mi] = sx[mw * 32 + mi * 16 + i16]; rstd[mi] = sx[128 + mw * 32 + mi * 16 + i16]; }
#pragma unroll
        for (int n = 0; n < NN; ++n) c4[n] = *(const f32x4*)(cs + 16 * (4 * NH + n) + 4 * g);
#pragma unroll
        for (int mi = 0; mi < 2; ++mi) {
            mean[mi] *= (1.0f / 576.0f);
            rstd[mi] = rsqrtf(fmaxf(rstd[mi] * (1.0f / 576.0f) - mean[mi] * mean[mi], 0.f) + LN_EPS);
        }
#pragma unroll
        for (int n = 0; n < NN; ++n)
#pragma unroll
            for (int pr = 0; pr < 2; ++pr)
#pragma unroll
                for (int mi = 0; mi < 2; ++mi) {
                    const float rs = MODE == 0 ? rstd[mi] * es[n][pr] : rstd[mi];
                    const unsigned pk = pk2((acc[mi][n][2 * pr] - mean[mi] * c4[n][2 * pr]) * rs,
                                            (acc[mi][n][2 * pr + 1] - mean[mi] * c4[n][2 * pr + 1]) * rs);
                    *(lds_u32)(ea[n][pr] + mi * 256) = pk;
                    if (MODE == 1) {
                        *(lds_u16)(ev[n][pr] + mi * 608) = (unsigned short)(pk & 0xffff);
                        *(lds_u16)(ev[n][pr] + mi * 608 + 16) = (unsigned short)(pk >> 16);
                    }
                }
    } else {
        f32x4 mean[2], rstd[2];
        float c1[NN];
#pragma unroll
        for (int mi = 0; mi < 2; ++mi) {
            mean[mi] = *(const f32x4*)(sx + mw * 32 + mi * 16 + 4 * g);
            rstd[mi] = *(const f32x4*)(sx + 128 + mw * 32 + mi * 16 + 4 * g);
        }
#pragma unroll
        for (int n = 0; n < NN; ++n) c1[n] = cs[16 * (4 * NH + n) + i16];
#pragma unroll
        for (int mi = 0; mi < 2; ++mi)
#pragma unroll
            for (int e = 0; e < 4; ++e) {
                mean[mi][e] *= (1.0f / 576.0f);
                rstd[mi][e] = rsqrtf(fmaxf(rstd[mi][e] * (1.0f / 576.0f) - mean[mi][e] * mean[mi][e], 0.f) + LN_EPS);
            }
#pragma unroll
        for (int n = 0; n < NN; ++n)
#pragma unroll
            for (int mi = 0; mi < 2; ++mi) {
                u32x2 o;
                o[0] = pk2((acc[mi][n][0] - mean[mi][0] * c1[n]) * rstd[mi][0], (acc[mi][n][1] - mean[mi][1] * c1[n]) * rstd[mi][1]);
                o[1] = pk2((acc[mi][n][2] - mean[mi][2] * c1[n]) * rstd[mi][2], (acc[mi][n][3] - mean[mi][3] * c1[n]) * rstd[mi][3]);
                *(lds_u32x2)(ea[n][0] + mi * 608) = o;
            }
    }
    __syncthreads();
    STAMP(0, 6);
    u32x4 cv[4];
#pragma unroll
    for (int t = 0; t < 4; ++t) cv[t] = *(const u32x4*)(smem + K1_EPI + (tid + 512 * t) * 16);
#pragma unroll
    for (int t = 0; t < 4; ++t)
        if (go[t] >= 0) st16wt(ws + go[t], cv[t]);
    STAMP(0, 7);
}

template <bool ISW>
DI void k1_loader(char* smem, const char* __restrict__ src, const char* __restrict__ wo, char* __restrict__ ws, int mt, int j, int l) {
    unsigned vo[2];
    __builtin_amdgcn_s_setprio(3);
    if (ISW) {
        const int g = l >> 4, i16 = l & 15;
#pragma unroll
        for (int t = 0; t < 2; ++t) {
            int u = (i16 >> 1) ^ (g | (t << 2)); u = u > 6 ? 6 : u;
            vo[t] = g * 3456 + u * 32 + (i16 & 1) * 16;
        }
        src += (216 * j) & ~15;
    } else {
        const int r8 = l >> 3, sl = l & 7;
#pragma unroll
        for (int t = 0; t < 2; ++t) vo[t] = r8 * 1152 + ((sl ^ ((r8 >> 1) | (t << 2))) << 4);
        src += (size_t)mt * 128 * 1152;
    }
    int kc = (2 * mt + j) % 9;
    auto issue = [&](int it) {
        char* st = smem + (it & 3) * 32768 + (ISW ? 16384 : 0);
        const char* sp = src + (ISW ? kc * 221184 : kc * 128);
#pragma unroll
        for (int i = 0; i < 16; ++i)
            glds16(sp + i * (ISW ? 13824 : 9216) + vo[ISW ? (i >> 1) & 1 : i & 1], st + i * 1024);
        kc = kc == 8 ? 0 : kc + 1;
    };
#pragma unroll 1
    for (int it = 0; it < 3; ++it) issue(it);
    asm volatile("s_waitcnt vmcnt(32)" ::: "memory");
    __builtin_amdgcn_s_barrier();
    issue(3);
    const int f0 = blockIdx.x * 2 + (ISW ? 1 : 0);
    const char* wp[2]; int st1[2], st2[2];
#pragma unroll
    for (int t = 0; t < 2; ++t) {
        const int fr = (t == 1 && f0 < 136) ? f0 + 512 : f0;
        const int nt = fr / 18, ks = fr - nt * 18, g = l >> 4;
        const int row0 = ks < 16 ? (2 * ks + (g >> 1)) * 18 + (g & 1) * 8 : ((ks - 16) * 16 + g * 4) * 18 + 16;
        wp[t] = wo + row0 * 1152 + (nt * 16 + (l & 15)) * 2;
        st1[t] = 1152; st2[t] = ks < 16 ? 1152 : 17 * 1152;
    }
    asm volatile("s_waitcnt vmcnt(32)" ::: "memory");
    __builtin_amdgcn_s_barrier();
    issue(4);
    unsigned wv[2][8];
#pragma unroll
    for (int t = 0; t < 2; ++t)
#pragma unroll
        for (int e = 0; e < 8; ++e) {
            if (t == 1 && e == 7) asm volatile("s_waitcnt vmcnt(62)" ::: "memory");
            asm volatile("global_load_ushort %0, %1, off" : "=v"(wv[t][e]) : "v"(wp[t]) : "memory");
            wp[t] += (e & 1) ? st2[t] : st1[t];
        }
#pragma unroll 1
    for (int it = 1; it < 4; ++it) {
        asm volatile("s_waitcnt vmcnt(47)" ::: "memory");
        __builtin_amdgcn_s_barrier();
        issue(it + 4);
    }
    asm volatile("s_waitcnt vmcnt(32)" ::: "memory");
    __builtin_amdgcn_s_barrier();
    issue(8);
    asm volatile("s_waitcnt vmcnt(32)" ::: "memory");
    __builtin_amdgcn_s_barrier();
    asm volatile("s_waitcnt vmcnt(16)" ::: "memory");
    __builtin_amdgcn_s_barrier();
    asm volatile("s_waitcnt vmcnt(0)" ::: "memory");
    __builtin_amdgcn_s_barrier();
    __builtin_amdgcn_sched_barrier(0);
#pragma unroll
    for (int t = 0; t < 2; ++t) {
        u32x4 v;
#pragma unroll
        for (int e = 0; e < 4; ++e) {
            asm volatile("" : "+v"(wv[t][2 * e]), "+v"(wv[t][2 * e + 1]));
            v[e] = wv[t][2 * e] | (wv[t][2 * e + 1] << 16);
        }
        if (t == 0 || f0 < 136) st16wt(ws + WS_WO + (size_t)(f0 + 512 * t) * 1024 + l * 16, v);
    }
#pragma unroll
    for (int t = 0; t < 4; ++t)
        __hip_atomic_store((gu64*)(ws + WS_GR + blockIdx.x * 4096 + ((ISW ? 4 : 0) + t) * 512 + l * 8), 0ull, RLX_AGENT);
    __builtin_amdgcn_s_barrier();
    __builtin_amdgcn_s_barrier();
}

__global__ __launch_bounds__(640) void k1_qkv(const char* __restrict__ x, const char* __restrict__ wqkv,
                                              const char* __restrict__ wo, char* __restrict__ ws) {
    __shared__ __attribute__((aligned(16))) char smem[K1_LDS];
    const int b = blockIdx.x, xcd = b & 7, i = b >> 3;
    const int mt = (xcd >> 1) * 4 + (i & 3), j = (xcd & 1) * 8 + (i >> 2);
    const int w = __builtin_amdgcn_readfirstlane(threadIdx.x >> 6), l = threadIdx.x & 63;
    if (w >= 8) {
        if (w == 8) k1_loader<false>(smem, x, wo, ws, mt, j, l);
        else k1_loader<true>(smem, wqkv, wo, ws, mt, j, l);
    } else if (w < 4) {
        if (j < 10) k1_body<0, 0>(smem, ws, mt, j, w, l);
        else if (j == 10) k1_body<1, 0>(smem, ws, mt, j, w, l);
        else k1_body<2, 0>(smem, ws, mt, j, w, l);
    } else {
        if (j < 10) k1_body<0, 1>(smem, ws, mt, j, w, l);
        else if (j == 10) k1_body<1, 1>(smem, ws, mt, j, w, l);
        else k1_body<2, 1>(smem, ws, mt, j, w, l);
    }
}

#define SB0 __builtin_amdgcn_sched_barrier(0)
#define EX(x) __builtin_amdgcn_exp2f(x)
#define MF(A, B, C) __builtin_amdgcn_mfma_f32_32x32x16_bf16(A, B, C, 0, 0, 0)
#define BC(x) __builtin_bit_cast(bf16x8, x)

DI f32x16 lo16(const f32x32& v) { return __builtin_shufflevector(v, v, 0, 1, 2, 3, 4, 5, 6, 7, 8, 9, 10, 11, 12, 13, 14, 15); }
DI f32x16 hi16(const f32x32& v) { return __builtin_shufflevector(v, v, 16, 17, 18, 19, 20, 21, 22, 23, 24, 25, 26, 27, 28, 29, 30, 31); }
DI bf16x8 ldsb8(const char* p) { return *(const bf16x8*)p; }

DI unsigned pkp(float a, float b) {
    unsigned r = __builtin_bit_cast(unsigned, b);
    asm("v_mov_b32_sdwa %0, %1 dst_sel:WORD_0 dst_unused:UNUSED_PRESERVE src0_sel:WORD_1" : "+v"(r) : "v"(a));
    return r;
}

struct AttnCarry { float e0, e1, e2, e3; u32x4 pc; bf16x8 v0, v1, v2, vl; };

template <int O> DI void ldv3(AttnCarry& cy, unsigned a, unsigned long long m) {
    asm volatile("s_mov_b64 exec, %4\n\tds_read_b128 %0, %3 offset:%5\n\tds_read_b128 %1, %3 offset:%6\n\t"
                 "ds_read_b128 %2, %3 offset:%7\n\ts_mov_b64 exec, -1"
                 : "+v"(cy.v0), "+v"(cy.v1), "+v"(cy.v2) : "v"(a), "s"(m), "n"(O), "n"(O + 608), "n"(O + 4864));
}
template <int O> DI void ldv1(AttnCarry& cy, unsigned a, unsigned long long m) {
    asm volatile("s_mov_b64 exec, %2\n\tds_read_b128 %0, %1 offset:%3\n\ts_mov_b64 exec, -1"
                 : "+v"(cy.vl) : "v"(a), "s"(m), "n"(O + 5472));
}

template <int PP>
DI void attn_step(const char* knext, unsigned vcur, const f32x16& sa, const f32x16& sb, f32x16& na, f32x16& nb,
                  f32x16& o, AttnCarry& cy, const bf16x8& q1, const s16x4& q2a, const s16x4& q2b, const f32x32& cinit,
                  const u32x4& k2n, unsigned long long vrow) {
    constexpr int VO = ((PP + 3) & 3) * 1216;
    s16x4 k2 = __builtin_bit_cast(s16x4, (PP & 2) ? __builtin_shufflevector(k2n, k2n, 2, 3) : __builtin_shufflevector(k2n, k2n, 0, 1));
    u32x4 pa, pb;
    float a0, a1, a2, a3, b0, b1, b2, b3;
#define EXA(s, i) a0 = EX(s[i]); a1 = EX(s[i + 1]); a2 = EX(s[i + 2]); a3 = EX(s[i + 3])
#define EXB(s, i) b0 = EX(s[i]); b1 = EX(s[i + 1]); b2 = EX(s[i + 2]); b3 = EX(s[i + 3])
    bf16x8 ka = ldsb8(knext), kb = ldsb8(knext + 4096);
    SB0;
    ldv3<VO>(cy, vcur, vrow);
    SB0;
    f32x32 n2 = __builtin_amdgcn_mfma_f32_32x32x4bf16_1k(k2, (PP & 1) ? q2b : q2a, cinit, 0, 0, 0);
    SB0;
    cy.pc[2] = pkp(cy.e0, cy.e1); cy.pc[3] = pkp(cy.e2, cy.e3);
    SB0;
    EXA(sa, 0);
    SB0;
    o = MF(cy.vl, BC(cy.pc), o);
    SB0;
    EXB(sa, 4);
    SB0;
    ldv1<VO>(cy, vcur, vrow);
    SB0;
    pa[0] = pkp(a0, a1); pa[1] = pkp(a2, a3);
    SB0;
    EXA(sa, 8);
    SB0;
    pa[2] = pkp(b0, b1); pa[3] = pkp(b2, b3);
    SB0;
    asm volatile("s_waitcnt lgkmcnt(1)");
    SB0;
    na = MF(ka, q1, lo16(n2));
    SB0;
    EXB(sa, 12);
    SB0;
    pb[0] = pkp(a0, a1); pb[1] = pkp(a2, a3);
    SB0;
    o = MF(cy.v0, BC(pa), o);
    SB0;
    EXA(sb, 0);
    SB0;
    pb[2] = pkp(b0, b1); pb[3] = pkp(b2, b3);
    SB0;
    asm volatile("s_waitcnt lgkmcnt(0)");
    SB0;
    nb = MF(kb, q1, hi16(n2));
    SB0;
    EXB(sb, 4);
    SB0;
    pa[0] = pkp(a0, a1); pa[1] = pkp(a2, a3);
    SB0;
    o = MF(cy.v1, BC(pb), o);
    SB0;
    EXA(sb, 8);
    SB0;
    pa[2] = pkp(b0, b1); pa[3] = pkp(b2, b3);
    SB0;
    cy.e0 = EX(sb[12]); cy.e1 = EX(sb[13]);
    SB0;
    o = MF(cy.v2, BC(pa), o);
    SB0;
    cy.e2 = EX(sb[14]); cy.e3 = EX(sb[15]);
    SB0;
    cy.pc[0] = pkp(a0, a1); cy.pc[1] = pkp(a2, a3);
    SB0;
#undef EXA
#undef EXB
}

template <bool FIRST>
DI f32x16 attn_pass(const char* smem, int l, const bf16x8& q1, const s16x4& q2a, const s16x4& q2b, const f32x32& cinit) {
    const int r = l & 31, h = l >> 5;
    const char* kp = smem + h * 512 + r * 16;
    unsigned vp = (unsigned)(uintptr_t)smem + 9216 + h * 304 + r * 16;
    const unsigned long long vrow = 0x0007ffff0007ffffull;
    f32x16 o = (f32x16)0.f;
    f32x16 sa, sb, ta, tb;
    AttnCarry cy;
    cy.e0 = cy.e1 = cy.e2 = cy.e3 = 0.f; cy.pc = (u32x4)0u;
    cy.v0 = cy.v1 = cy.v2 = cy.vl = BC((u32x4)0u);
    u32x4 k2 = *(const u32x4*)(kp + 8192);
    {
        s16x4 k2p = __builtin_bit_cast(s16x4, __builtin_shufflevector(k2, k2, 0, 1));
        f32x32 n2 = __builtin_amdgcn_mfma_f32_32x32x4bf16_1k(k2p, q2a, cinit, 0, 0, 0);
        sa = MF(ldsb8(kp), q1, lo16(n2));
        sb = MF(ldsb8(kp + 4096), q1, hi16(n2));
    }
#pragma unroll 1
    for (int c = 0; c < 8; ++c) {
        if (c == 4) __builtin_amdgcn_s_setprio(2);
        else if (c == 6) __builtin_amdgcn_s_setprio(1);
        else if (c == 7) __builtin_amdgcn_s_setprio(0);
        attn_step<1>(kp + 1024, vp, sa, sb, ta, tb, o, cy, q1, q2a, q2b, cinit, k2, vrow);
        attn_step<2>(kp + 2048, vp, ta, tb, sa, sb, o, cy, q1, q2a, q2b, cinit, k2, vrow);
        attn_step<3>(kp + 3072, vp, sa, sb, ta, tb, o, cy, q1, q2a, q2b, cinit, k2, vrow);
        if (FIRST && c == 0) {
            asm volatile("s_waitcnt vmcnt(0)" ::: "memory");
            __builtin_amdgcn_s_barrier();
        }
        k2 = *(const u32x4*)(kp + KCH + 8192);
        attn_step<0>(kp + KCH, vp, ta, tb, sa, sb, o, cy, q1, q2a, q2b, cinit, k2, vrow);
        kp += KCH; vp += KCH;
    }
    cy.pc[2] = pkp(cy.e0, cy.e1); cy.pc[3] = pkp(cy.e2, cy.e3);
    SB0;
    asm volatile("s_nop 1");
    SB0;
    o = MF(cy.vl, BC(cy.pc), o);
    return o;
}

DI void attn_wave(char* smem, char* __restrict__ ws, int head, int qb, int w, int l) {
    const int r = l & 31, h = l >> 5;
    const int qt = qb * 8 + w;
    STAMP(1, 0);
    __builtin_amdgcn_s_setprio(3);
    const char* qp = ws + WS_Q + (size_t)head * QIMG + (size_t)qb * QCH;
    bf16x8 q1; unsigned q2;
    asm volatile("global_load_dwordx4 %0, %1, off" : "=v"(q1) : "v"(qp + w * 1024 + h * 512 + r * 16) : "memory");
    asm volatile("global_load_dword %0, %1, off" : "=v"(q2) : "v"(qp + 8192 + (w >> 2) * 512 + r * 16 + (w & 3) * 4) : "memory");
    SB0;
    {
        const char* src = ws + WS_KV + (size_t)head * KVIMG + w * 1024 + l * 16;
        char* dst = smem + w * 1024;
#pragma unroll
        for (int t = 0; t < 19; ++t) glds16(src + t * 8192, dst + t * 8192);
    }
    asm volatile("s_waitcnt vmcnt(16)" ::: "memory");
    SB0;
    asm volatile("" : "+v"(q1), "+v"(q2));
    u32x2 qa; qa[0] = q2; qa[1] = 0u;
    u32x2 qbv; qbv[0] = 0u; qbv[1] = q2;
    const s16x4 q2a = __builtin_bit_cast(s16x4, qa), q2b = __builtin_bit_cast(s16x4, qbv);
    f32x32 cinit = (f32x32)0.f;
    __builtin_amdgcn_s_barrier();
    SB0;
    STAMP(1, 2);
    STAMPC(1, 14);
    f32x16 o = attn_pass<true>(smem, l, q1, q2a, q2b, cinit);
    STAMP(1, 3);
    STAMPC(1, 15);
    auto denom = [&]() {
        const float o10 = o[10];
        const unsigned lb = __builtin_bit_cast(unsigned, o10);
        auto sw = __builtin_amdgcn_permlane32_swap(lb, lb, false, false);
        const unsigned l0 = sw[0];
        return __builtin_bit_cast(float, l0);
    };
    float lsum = denom();
    bool ok = (lsum >= 9.0e-13f) && (lsum <= 1.0e18f);
#pragma unroll
    for (int e = 0; e < 8; ++e) ok = ok && (fabsf(o[e]) <= 3.0e38f);
    ok = ok && (h == 1 || ((fabsf(o[8]) <= 3.0e38f) && (fabsf(o[9]) <= 3.0e38f)));
    if (!__all(ok)) {
        const char* kp = smem + h * 512 + r * 16;
        float m = -3.0e38f;
#pragma unroll 1
        for (int c = 0; c < 8; ++c) {
            u32x4 k2 = *(const u32x4*)(kp + 8192);
#pragma unroll
            for (int pp = 0; pp < 4; ++pp) {
                s16x4 k2p = __builtin_bit_cast(s16x4, (pp & 2) ? __builtin_shufflevector(k2, k2, 2, 3) : __builtin_shufflevector(k2, k2, 0, 1));
                f32x32 n2 = __builtin_amdgcn_mfma_f32_32x32x4bf16_1k(k2p, (pp & 1) ? q2b : q2a, cinit, 0, 0, 0);
                f32x16 sa = MF(ldsb8(kp + pp * 1024), q1, lo16(n2));
                f32x16 sb = MF(ldsb8(kp + pp * 1024 + 4096), q1, hi16(n2));
#pragma unroll
                for (int e = 0; e < 16; ++e) m = fmaxf(m, fmaxf(sa[e], sb[e]));
            }
            kp += KCH;
        }
        m = fmaxf(m, __shfl_xor(m, 32));
        cinit = (f32x32)(-m);
        o = attn_pass<false>(smem, l, q1, q2a, q2b, cinit);
        lsum = denom();
    }
    const float inv = 1.0f / lsum;
    char* op = ws + WS_O + ((size_t)(qt * 2 + (r >> 4)) * 18) * 1024;
    u32x2 a, c;
    a[0] = pk2(o[0] * inv, o[1] * inv); a[1] = pk2(o[2] * inv, o[3] * inv);
    c[0] = pk2(o[4] * inv, o[5] * inv); c[1] = pk2(o[6] * inv, o[7] * inv);
    char* mp = op + (head >> 1) * 1024 + (head & 1) * 512 + (r & 15) * 16 + h * 8;
    st8wt(mp, a);
    st8wt(mp + 256, c);
    if (h == 0) st4wt(op + (16 + (head >> 4)) * 1024 + ((head >> 2) & 3) * 256 + (r & 15) * 16 + (head & 3) * 4, pk2(o[8] * inv, o[9] * inv));
    asm volatile("s_waitcnt vmcnt(0)" ::: "memory");
    if (l == 0) __hip_atomic_fetch_add((gu32*)(ws + WS_CNT + qt * 128), 1u, RLX_AGENT);
    STAMP(1, 4);
}

__global__ __launch_bounds__(576) void k2_attn(const char* __restrict__ x, const char* __restrict__ wr, const char* __restrict__ wg,
                                               const char* __restrict__ wu, const char* __restrict__ wd, char* __restrict__ ws,
                                               char* __restrict__ out) {
    __shared__ __attribute__((aligned(16))) char smem[163840];
    const int b = blockIdx.x, xcd = b & 7, i = b >> 3;
    const int tid = threadIdx.x;
    const int w = __builtin_amdgcn_readfirstlane(tid >> 6), l = tid & 63;
    const int mb = xcd * 8 + (i >> 2), ns = i & 3;
    if (w == 8) {
        __builtin_amdgcn_s_barrier();
        __builtin_amdgcn_s_barrier();
        char* sink = smem + KVIMG;
#pragma unroll 1
        for (int t = 0; t < 21; ++t) {
            int P = t * 32 + i; P = P < 648 ? P : 647;
            const int u = P * 64 + l;
            const int row = ((u >> 3) * 7282) >> 16;
            const unsigned o = row * 9216 + xcd * 1152 + (u - row * 72) * 16;
            glds16(wg + o, sink);
            glds16(wu + o, sink + 1024);
            glds16(wd + xcd * 663552 + u * 16, sink + 2048);
            glds16(ws + WS_WO + u * 16, sink + 3072);
            asm volatile("s_waitcnt vmcnt(48)" ::: "memory");
        }
#pragma unroll 1
        for (int t = 0; t < 9; ++t) glds16(x + xcd * 294912 + (t * 32 + i) * 1024 + l * 16, sink + 4096);
        asm volatile("s_waitcnt vmcnt(0)" ::: "memory");
    } else {
        attn_wave(smem, ws, xcd * 4 + (i >> 3), i & 7, w, l);
    }
    SB0;
    const int g = l >> 4, i16 = l & 15;
    const int nt = ns * 9 + w;
    const int c0 = nt * 16 + 4 * g;
    const char* wsrc = ws + WS_WO + (size_t)nt * 18432 + l * 16;
    bf16x8 wf[18];
#pragma unroll
    for (int ks = 0; ks < 18; ++ks) wf[ks] = *(const bf16x8*)(wsrc + ks * 1024);
    u32x2 xr[2];
#pragma unroll
    for (int mi = 0; mi < 2; ++mi) xr[mi] = *(const u32x2*)(x + (size_t)(mb * 32 + mi * 16 + i16) * 1152 + c0 * 2);
    const u32x2 wrv = *(const u32x2*)(wr + c0 * 2);
    float wsum = bf2f(((const unsigned short*)wr)[tid]);
    SB0;
    if (w == 8) {
        for (unsigned spins = 0; spins < SPIN_CAP; ++spins) {
            if (__builtin_amdgcn_readfirstlane(__hip_atomic_load((gu32*)(ws + WS_CNT + mb * 128), RLX_AGENT)) >= 32u) break;
            __builtin_amdgcn_s_sleep(2);
        }
    }
    STAMP(2, 0);
    asm volatile("" ::: "memory");
    __builtin_amdgcn_s_barrier();
    asm volatile("" ::: "memory");
    STAMP(2, 6);
    {
        const char* osrc = ws + WS_O + (size_t)mb * 36864 + w * 1024 + l * 16;
        u32x4 ot[4];
#pragma unroll
        for (int t = 0; t < 4; ++t) asm volatile("global_load_dwordx4 %0, %1, off sc1" : "=v"(ot[t]) : "v"(osrc + t * 9216) : "memory");
        asm volatile("s_waitcnt vmcnt(0)" : "+v"(ot[0]), "+v"(ot[1]), "+v"(ot[2]), "+v"(ot[3]) :: "memory");
#pragma unroll
        for (int t = 0; t < 4; ++t) *(u32x4*)(smem + w * 1024 + l * 16 + t * 9216) = ot[t];
    }
    __syncthreads();
    STAMP(2, 1);
    f32x4 acc[2] = {(f32x4)0.f, (f32x4)0.f};
#pragma unroll
    for (int ks = 0; ks < 18; ++ks) {
#pragma unroll
        for (int mi = 0; mi < 2; ++mi) {
            bf16x8 of = *(const bf16x8*)(smem + (mi * 18 + ks) * 1024 + l * 16);
            acc[mi] = __builtin_amdgcn_mfma_f32_16x16x32_bf16(wf[ks], of, acc[mi], 0, 0, 0);
        }
    }
    STAMP(2, 2);
    char* ht = smem + 36864;
    float* red = (float*)(smem + 36864 + 9728);
    float* tot = red + 9 * 96;
    float* wsm = tot + 96;
    float* sm = wsm + 16;
    {
        const float w0 = bflo(wrv[0]), w1 = bfhi(wrv[0]), w2 = bflo(wrv[1]), w3 = bfhi(wrv[1]);
        float s[2], qq[2], e[2];
#pragma unroll
        for (int mi = 0; mi < 2; ++mi) {
            u32x2 hv;
            hv[0] = pk2(bflo(xr[mi][0]) + rbf(acc[mi][0]), bfhi(xr[mi][0]) + rbf(acc[mi][1]));
            hv[1] = pk2(bflo(xr[mi][1]) + rbf(acc[mi][2]), bfhi(xr[mi][1]) + rbf(acc[mi][3]));
            *(u32x2*)(ht + (mi * 16 + i16) * 304 + w * 32 + g * 8) = hv;
            const float h0 = bflo(hv[0]), h1 = bfhi(hv[0]), h2 = bflo(hv[1]), h3 = bfhi(hv[1]);
            s[mi] = (h0 + h1) + (h2 + h3);
            qq[mi] = (h0 * h0 + h1 * h1) + (h2 * h2 + h3 * h3);
            e[mi] = (h0 * w0 + h1 * w1) + (h2 * w2 + h3 * w3);
        }
        const float s1 = sw16(s[0], s[1]), q1 = sw16(qq[0], qq[1]), e1 = sw16(e[0], e[1]);
        red[w * 96 + l] = sw32(s1, q1);
        const float e2 = sw32(e1, e1);
        if (l < 32) red[w * 96 + 64 + l] = e2;
        wsum += rorf<1>(wsum); wsum += rorf<2>(wsum); wsum += rorf<4>(wsum); wsum += rorf<8>(wsum);
        wsum = sw16(wsum, wsum); wsum = sw32(wsum, wsum);
        if (l == 0) wsm[w] = wsum;
    }
    __syncthreads();
    STAMP(2, 3);
    if (tid < 96) {
        float a = 0.f;
#pragma unroll
        for (int ww = 0; ww < 9; ++ww) a += red[ww * 96 + tid];
        char* gr = ws + WS_GR3 + (size_t)mb * 3072 + tid * 8;
        put_granule(gr + ns * 768, a);
        float v[4];
        get_granules<4>(gr, 768, v);
        tot[tid] = (v[0] + v[1]) + (v[2] + v[3]);
    }
    const int row = tid / 18, ch = tid - row * 18;
    const u32x4 hv = *(const u32x4*)(ht + row * 304 + ch * 16);
    char* optr = out + (size_t)(mb * 32 + row) * 1152 + ns * 288 + ch * 16;
    float wt = 0.f;
#pragma unroll
    for (int ww = 0; ww < 9; ++ww) wt += wsm[ww];
    __syncthreads();
    STAMP(2, 4);
    int br;
    {
        const int r = l & 31;
        const float mean = tot[r] * (1.0f / 576.0f);
        const float rstd = rsqrtf(fmaxf(tot[32 + r] * (1.0f / 576.0f) - mean * mean, 0.f) + LN_EPS);
        const float rv = (tot[64 + r] - mean * wt) * rstd;
        br = argmax_lane(rv);
        if (ns == 0 && w == 0 && l == br) {
            char* cg = ws + WS_CANDG + mb * 8;
            put_granule(cg, rv); put_granule_u(cg + 512, mb * 32 + r); put_granule(cg + 1024, mean); put_granule(cg + 1536, rstd);
        }
    }
    if (row == br) {
#pragma unroll
        for (int k = 0; k < 4; ++k) put_granule_u(ws + WS_ROWG + ((size_t)mb * 288 + ns * 72 + ch * 4 + k) * 8, hv[k]);
    }
    u32x4 ov;
    {
        const float mean = tot[row] * (1.0f / 576.0f);
        const float rstd = rsqrtf(fmaxf(tot[32 + row] * (1.0f / 576.0f) - mean * mean, 0.f) + LN_EPS);
#pragma unroll
        for (int k = 0; k < 4; ++k) {
            const float a = bflo(hv[k]), c = bfhi(hv[k]);
            ov[k] = pk2(a + rbf((a - mean) * rstd), c + rbf((c - mean) * rstd));
        }
        if (row != br) *(u32x4*)optr = ov;
    }
    STAMP(2, 5);
    const int f = (b & 7) * 18 + (b >> 3);
    const int kr = tid >> 2, cq = tid & 3, rg = l >> 3;
    u32x4 gv[4], uv[4], dv[4];
    if (b < 144) {
#pragma unroll
        for (int t = 0; t < 4; ++t) {
            gv[t] = *(const u32x4*)(wg + (size_t)(kr + 144 * t) * (F_ * 2) + f * 64 + cq * 16);
            uv[t] = *(const u32x4*)(wu + (size_t)(kr + 144 * t) * (F_ * 2) + f * 64 + cq * 16);
        }
#pragma unroll
        for (int t = 0; t < 4; ++t) dv[t] = *(const u32x4*)(wd + (size_t)(f * 32 + rg + 8 * t) * 1152 + w * 128 + (l & 7) * 16);
    } else if (w != (br * 18 >> 6) && w != (br * 18 + 17 >> 6)) {
        return;
    }
    SB0;
    STAMP(3, 0);
    float cand[4];
    get_granules<4>(ws + WS_CANDG + l * 8, 512, cand);
    const int bl = argmax_lane(cand[0]);
    const float best = __builtin_bit_cast(float, __builtin_amdgcn_readlane(__builtin_bit_cast(int, cand[0]), bl));
    const int bidx = __builtin_amdgcn_readlane(__builtin_bit_cast(int, cand[1]), bl) & (S_ - 1);
    const float bmean = __builtin_bit_cast(float, __builtin_amdgcn_readlane(__builtin_bit_cast(int, cand[2]), bl));
    const float brstd = __builtin_bit_cast(float, __builtin_amdgcn_readlane(__builtin_bit_cast(int, cand[3]), bl));
    if (row == br && bl != mb) *(u32x4*)optr = ov;
    if (b >= 144) return;
    STAMP(3, 1);
    float hr[4], hf[1];
    get_granules<4>(ws + WS_ROWG + ((size_t)bl * 288 + (kr >> 1)) * 8, 72 * 8, hr);
    float ga[8], ua[8];
#pragma unroll
    for (int e = 0; e < 8; ++e) { ga[e] = 0.f; ua[e] = 0.f; }
#pragma unroll
    for (int t = 0; t < 4; ++t) {
        const unsigned hp = __builtin_bit_cast(unsigned, hr[t]);
        const float xs = rbf((((kr & 1) ? bfhi(hp) : bflo(hp)) - bmean) * brstd);
#pragma unroll
        for (int e = 0; e < 4; ++e) {
            ga[2 * e] += xs * bflo(gv[t][e]); ga[2 * e + 1] += xs * bfhi(gv[t][e]);
            ua[2 * e] += xs * bflo(uv[t][e]); ua[2 * e + 1] += xs * bfhi(uv[t][e]);
        }
    }
    STAMP(3, 2);
    {
        float v[8]; f32x4 z;
#pragma unroll
        for (int e = 0; e < 8; ++e) v[e] = sw32(ga[e], ua[e]);
#pragma unroll
        for (int e = 0; e < 4; ++e) { float t = sw16(v[e], v[e + 4]); t += rorf<8>(t); t += rorf<4>(t); z[e] = t; }
        if ((l & 12) == 0) *(f32x4*)(sm + w * 64 + (l >> 5) * 32 + cq * 8 + ((l >> 4) & 1) * 4) = z;
    }
    __syncthreads();
    STAMP(3, 3);
    float act;
    {
        float a = 0.f;
#pragma unroll
        for (int p = 0; p < 9; ++p) a += sm[p * 64 + l];
        const unsigned ab = __builtin_bit_cast(unsigned, rbf(a));
        auto r = __builtin_amdgcn_permlane32_swap(ab, ab, false, false);
        const unsigned r0 = r[0], r1 = r[1];
        const float gt = __builtin_bit_cast(float, r0), up = __builtin_bit_cast(float, r1);
        act = rbf(rbf(gt / (1.0f + __expf(-gt))) * up);
    }
    float ya[8];
#pragma unroll
    for (int e = 0; e < 8; ++e) ya[e] = 0.f;
#pragma unroll
    for (int t = 0; t < 4; ++t) {
        const float a = __shfl(act, rg + 8 * t);
#pragma unroll
        for (int e = 0; e < 4; ++e) { ya[2 * e] += a * bflo(dv[t][e]); ya[2 * e + 1] += a * bfhi(dv[t][e]); }
    }
    {
        float z[4];
#pragma unroll
        for (int e = 0; e < 4; ++e) z[e] = sw32(ya[e], ya[e + 4]);
        float u0 = sw16(z[0], z[2]), u1 = sw16(z[1], z[3]);
        u0 += rorf<8>(u0); u1 += rorf<8>(u1);
        put_granule(ws + WS_GR4 + ((size_t)f * 576 + w * 64 + (l & 7) * 8 + rg) * 8, (l & 8) ? u1 : u0);
    }
    STAMP(3, 4);
    if (b < 36) {
        get_granules<1>(ws + WS_ROWG + ((size_t)bl * 288 + b * 8 + ((l & 15) >> 1)) * 8, 0, hf);
        float v[4];
        get_granules<4>(ws + WS_GR4 + ((size_t)(tid >> 4) * 576 + b * 16 + (l & 15)) * 8, 36 * 576 * 8, v);
        float a = (v[0] + v[1]) + (v[2] + v[3]);
        a = sw16(a, a); a = sw32(a, a);
        if (l < 16) sm[576 + w * 16 + l] = a;
        __syncthreads();
        if (tid < 16) {
            float y = 0.f;
#pragma unroll
            for (int t = 0; t < 9; ++t) y += sm[576 + t * 16 + tid];
            const unsigned hp = __builtin_bit_cast(unsigned, hf[0]);
            const float hv1 = (tid & 1) ? bfhi(hp) : bflo(hp);
            const float sg = rbf(1.0f / (1.0f + __expf(-rbf(best))));
            const float hn = rbf((hv1 - bmean) * brstd);
            const float upd = rbf(rbf(y) * sg);
            __bf16 o = (__bf16)(hv1 + rbf(hn + upd));
            ((unsigned short*)out)[(size_t)bidx * 576 + b * 16 + tid] = __builtin_bit_cast(unsigned short, o);
        }
        STAMP(3, 5);
    }
}

extern "C" void kernel_launch(void* const* d_in, const int* in_sizes, int n_in,
                              void* d_out, int out_size, void* d_ws, size_t ws_size,
                              hipStream_t stream) {
    const char* x = (const char*)d_in[0];
    const char* wqkv = (const char*)d_in[1];
    const char* wo = (const char*)d_in[2];
    const char* wr = (const char*)d_in[4];
    const char* wg = (const char*)d_in[5];
    const char* wu = (const char*)d_in[6];
    const char* wd = (const char*)d_in[7];
    char* ws = (char*)d_ws;
    char* out = (char*)d_out;
    k1_qkv<<<256, 640, 0, stream>>>(x, wqkv, wo, ws);
    k2_attn<<<256, 576, 0, stream>>>(x, wr, wg, wu, wd, ws, out);
}
```

```cpp
#include <hip/hip_runtime.h>
#include <hip/hip_bf16.h>
#include <stdint.h>

typedef __attribute__((ext_vector_type(4))) float f32x4;
typedef __attribute__((ext_vector_type(16))) float f32x16;
typedef __attribute__((ext_vector_type(32))) float f32x32;
typedef __attribute__((ext_vector_type(8))) __bf16 bf16x8;
typedef __attribute__((ext_vector_type(2))) __bf16 bf16x2;
typedef __attribute__((ext_vector_type(4))) short s16x4;
typedef __attribute__((ext_vector_type(8))) short s16x8;
typedef __attribute__((ext_vector_type(4))) unsigned u32x4;
typedef __attribute__((ext_vector_type(2))) unsigned u32x2;
typedef __attribute__((ext_vector_type(2))) float f32x2;
typedef __attribute__((address_space(3))) s16x4* lds_s16x4_ptr;

#define DI __device__ __forceinline__

#define STAMP(kid, i)
#define STAMPC(kid, i)

constexpr int S_ = 2048, D_ = 576, F_ = 4608;
constexpr float LN_EPS = 1e-5f;

constexpr int QCH = 9216;
constexpr int KCH = 18944;
constexpr int QIMG = 8 * QCH;
constexpr int KVIMG = 155648;
constexpr size_t WS_Q = 0;
constexpr size_t WS_KV = WS_Q + (size_t)32 * QIMG;
constexpr size_t WS_O = WS_KV + (size_t)32 * KVIMG;
constexpr size_t WS_WO = WS_O + (size_t)128 * 18 * 1024;
constexpr size_t WS_GR = WS_WO + (size_t)36 * 18 * 1024;
constexpr size_t WS_GR3 = WS_GR;
constexpr size_t WS_GR4 = WS_GR + (size_t)256 * 768;
constexpr size_t WS_ROWG = WS_GR4 + (size_t)144 * 576 * 8;
constexpr size_t WS_CANDG = WS_ROWG + (size_t)64 * 288 * 8;
constexpr size_t WS_SCORE = WS_CANDG + 2048;
static_assert(WS_SCORE + 16 * 2304 <= WS_GR + (1u << 20), "hand-off area");
constexpr unsigned O_EMPTY = 0xffffffffu;

#define RSLEEP 4
#define CSLEEP 2
typedef __attribute__((address_space(1))) unsigned gu32;
typedef __attribute__((address_space(1))) unsigned long long gu64;
#define RLX_AGENT __ATOMIC_RELAXED, __HIP_MEMORY_SCOPE_AGENT
constexpr unsigned SPIN_CAP = 1u << 20;
DI void put_granule_u(char* p, unsigned v) { __hip_atomic_store((gu64*)p, (1ull << 32) | v, RLX_AGENT); }
DI void put_granule(char* p, float v) { put_granule_u(p, __builtin_bit_cast(unsigned, v)); }
template <int N, int SLEEP = 1>
DI void get_granules(const char* p, int stride, float (&v)[N]) {
    for (unsigned spins = 0; spins < SPIN_CAP; ++spins) {
        bool ok = true;
#pragma unroll
        for (int k = 0; k < N; ++k) {
            const unsigned long long x = __hip_atomic_load((gu64*)(p + k * stride), RLX_AGENT);
            v[k] = __builtin_bit_cast(float, (unsigned)x); ok &= (x >> 32) == 1ull;
        }
        if (__all(ok)) break;
        __builtin_amdgcn_s_sleep(SLEEP);
    }
}
DI float sw32(float a, float b) {
    auto r = __builtin_amdgcn_permlane32_swap(__builtin_bit_cast(unsigned, a), __builtin_bit_cast(unsigned, b), false, false);
    const unsigned r0 = r[0], r1 = r[1];
    return __builtin_bit_cast(float, r0) + __builtin_bit_cast(float, r1);
}
DI float sw16(float a, float b) {
    auto r = __builtin_amdgcn_permlane16_swap(__builtin_bit_cast(unsigned, a), __builtin_bit_cast(unsigned, b), false, false);
    const unsigned r0 = r[0], r1 = r[1];
    return __builtin_bit_cast(float, r0) + __builtin_bit_cast(float, r1);
}
template <int R> DI unsigned ror(unsigned v) { return (unsigned)__builtin_amdgcn_update_dpp(0, (int)v, 0x120 + R, 0xf, 0xf, false); }
template <int R> DI float rorf(float v) { return __builtin_bit_cast(float, ror<R>(__builtin_bit_cast(unsigned, v))); }
DI int argmax_lane(float v) {
    const unsigned u = __builtin_bit_cast(unsigned, v);
    const unsigned key = (u & 0x80000000u) ? ~u : (u | 0x80000000u);
    unsigned m = key;
    m = max(m, ror<1>(m)); m = max(m, ror<2>(m)); m = max(m, ror<4>(m)); m = max(m, ror<8>(m));
    { auto r = __builtin_amdgcn_permlane16_swap(m, m, false, false); const unsigned r0 = r[0], r1 = r[1]; m = max(r0, r1); }
    { auto r = __builtin_amdgcn_permlane32_swap(m, m, false, false); const unsigned r0 = r[0], r1 = r[1]; m = max(r0, r1); }
    return __builtin_ctzll(__ballot(key == m));
}

DI float bf2f(unsigned short v) { return __builtin_bit_cast(float, (unsigned)v << 16); }
DI float bflo(unsigned v) { return __builtin_bit_cast(float, v << 16); }
DI float bfhi(unsigned v) { return __builtin_bit_cast(float, v & 0xffff0000u); }
DI unsigned pk2(float a, float b) {
    bf16x2 r; r[0] = (__bf16)a; r[1] = (__bf16)b;
    return __builtin_bit_cast(unsigned, r);
}
DI float rbf(float a) { return (float)(__bf16)a; }

#define OSLEEP 1
#define WBATCH 3
#define WT " sc1"
DI void st16wt(void* p, const u32x4& v) { asm volatile("global_store_dwordx4 %0, %1, off" WT "\n\ts_nop 2" :: "v"(p), "v"(v) : "memory"); }
DI void st8wt(void* p, const u32x2& v) { asm volatile("global_store_dwordx2 %0, %1, off" WT "\n\ts_nop 2" :: "v"(p), "v"(v) : "memory"); }
DI void st4wt(void* p, unsigned v) { asm volatile("global_store_dword %0, %1, off" WT :: "v"(p), "v"(v) : "memory"); }

DI unsigned cnt_addr(char* smem) { return (unsigned)(size_t)(__attribute__((address_space(3))) char*)(smem + 163836); }
DI unsigned cnt_peek(char* smem) {
    unsigned v;
    asm volatile("ds_read_b32 %0, %1\n\ts_waitcnt lgkmcnt(0)" : "=v"(v) : "v"(cnt_addr(smem)) : "memory");
    return __builtin_amdgcn_readfirstlane(v);
}
DI void glds16(const void* g, void* lds) {
    __builtin_amdgcn_global_load_lds((const __attribute__((address_space(1))) unsigned*)g,
                                     (__attribute__((address_space(3))) unsigned*)lds, 16, 0, 0);
}

constexpr int K1_EPI = 131072;
constexpr int K1_CS = K1_EPI + 6 * 4864;
constexpr int K1_ST = K1_CS + 512;
constexpr int K1_DUM = K1_ST + 1024;
constexpr int K1_LDS = 163840;

struct K1Frag { bf16x8 x[2][2]; bf16x8 w[2][5]; };
typedef __attribute__((address_space(3))) unsigned* lds_u32;
typedef __attribute__((address_space(3))) unsigned short* lds_u16;
typedef __attribute__((address_space(3))) u32x2* lds_u32x2;

template <int MODE, int NH>
DI void k1_body(char* smem, char* __restrict__ ws, int mt, int j, int w, int l) {
    constexpr bool SWAP = MODE != 2;
    constexpr int NW = NH ? 5 : 4, NN = NH ? 3 : 4;
    const int g = l >> 4, i16 = l & 15;
    const int mw = w & 3;
    STAMP(0, 0);
    f32x4 acc[2][4], asq = (f32x4)0.f;
#pragma unroll
    for (int n = 0; n < 4; ++n) { acc[0][n] = (f32x4)0.f; acc[1][n] = (f32x4)0.f; }
    bf16x8 ones;
#pragma unroll
    for (int e = 0; e < 8; ++e) ones[e] = (__bf16)1.0f;

    const int q = i16 >> 2, p = i16 & 3;
    const int fl = q | ((g & 1) << 2);
    const unsigned sbase = (unsigned)(uintptr_t)smem;
    unsigned xa[2];
    xa[0] = sbase + mw * 4096 + i16 * 128 + ((g ^ (i16 >> 1)) << 4); xa[1] = xa[0] ^ 64;
    const unsigned wrow = sbase + 16384 + (8 * g + q) * 256 + p * 8;
    unsigned wa[NW];
#pragma unroll
    for (int n = 0; n < NN; ++n) wa[n] = wrow + (((4 * NH + n) ^ fl) << 5);
    if (NH) { wa[3] = wrow + (((2 * mw) ^ fl) << 5); wa[NW - 1] = wrow + (((2 * mw + 1) ^ fl) << 5); }
    const int odd = j & 1, off = odd * 4;
    const bool patch = NH ? (!odd && i16 >= 12) : (odd && i16 < 4);

    const int tid = w * 64 + l;
    unsigned ea[NN][2], ev[NN][2]; float es[NN][2];
    const unsigned epi = sbase + K1_EPI, dum = sbase + K1_DUM + l * 8;
#pragma unroll
    for (int n = 0; n < NN; ++n) {
#pragma unroll
        for (int pr = 0; pr < (SWAP ? 2 : 1); ++pr) {
            const int col = SWAP ? 16 * (4 * NH + n) + 4 * g + 2 * pr - off : 16 * (4 * NH + n) + i16 - off;
            const bool valid = (col >= 0) && (col < 108);
            const int sI = (col * 57) >> 10;
            const int d = col - 18 * sI;
            const int gs = 6 * j + sI;
            if (SWAP) {
                const unsigned qk = epi + sI * 4864 + i16 * 16 + (d < 16 ? mw * 1024 + (d >> 3) * 512 + (d & 7) * 2 : 4096 + mw * 4);
                ea[n][pr] = (valid && gs < 64) ? qk : dum;
                asm volatile("" : "+v"(ea[n][pr]));
                if (MODE == 0) {
                    es[n][pr] = gs < 32 ? 0.23570226039551584f * 1.4426950408889634f : 1.0f;
                    asm volatile("" : "+v"(es[n][pr]));
                } else {
                    const unsigned vv = epi + sI * 4864 + mw * 1216 + ((i16 >> 2) & 1) * 304 + d * 16 + (((i16 >> 3) << 2) | (i16 & 3)) * 2;
                    ev[n][pr] = (valid && gs >= 64) ? vv : dum;
                    asm volatile("" : "+v"(ev[n][pr]));
                }
            } else {
                const unsigned vv = epi + sI * 4864 + mw * 1216 + (g & 1) * 304 + d * 16 + (g >> 1) * 8;
                ea[n][0] = valid ? vv : dum;
                asm volatile("" : "+v"(ea[n][0]));
            }
        }
    }
    int go[4];
    const int ch = mt >> 1, hb = mt & 1;
#pragma unroll
    for (int t = 0; t < 4; ++t) {
        const int un = tid + 512 * t;
        const int sI = (un * 3450) >> 20;
        const int o = (un - sI * 304) * 16;
        const int gs = 6 * j + sI, part = gs >> 5, head = gs & 31;
        const int base = part == 0 ? (int)WS_Q + head * QIMG + ch * QCH : (int)WS_KV + head * KVIMG + ch * KCH;
        const int dst = part == 2 ? 9216 + hb * 4864 + o : (o < 4096 ? hb * 4096 + o : 4096 + hb * 512 + o);
        go[t] = (un < 6 * 304 && (part == 2 || o < 4608)) ? base + dst : -1;
        asm volatile("" : "+v"(go[t]));
    }
    if (MODE != 0 && tid < 96 && 6 * j + (tid >> 4) >= 64)
        *(u32x4*)(smem + K1_EPI + (tid >> 4) * 4864 + ((tid >> 1) & 7) * 608 + (tid & 1) * 304 + 288) = (u32x4)0x3f803f80u;

    typedef __attribute__((address_space(3))) const bf16x8* lds_b8;
    auto rd = [&](K1Frag& f, unsigned so, int i) {
        const int kk = i / (NW + 1), t = i % (NW + 1);
        if (t == 0) {
            f.x[kk][0] = *(lds_b8)(xa[kk] + so);
            f.x[kk][1] = *(lds_b8)(xa[kk] + so + 2048);
        } else {
            s16x4 lo = __builtin_amdgcn_ds_read_tr16_b64_v4i16((lds_s16x4_ptr)(wa[t - 1] + so + kk * 8192));
            s16x4 hi = __builtin_amdgcn_ds_read_tr16_b64_v4i16((lds_s16x4_ptr)(wa[t - 1] + so + kk * 8192 + 1024));
            f.w[kk][t - 1] = __builtin_bit_cast(bf16x8, __builtin_shufflevector(lo, hi, 0, 1, 2, 3, 4, 5, 6, 7));
        }
    };
    auto mm = [&](const bf16x8& wf, const bf16x8& xf, f32x4& c) {
        c = SWAP ? __builtin_amdgcn_mfma_f32_16x16x32_bf16(wf, xf, c, 0, 0, 0)
                 : __builtin_amdgcn_mfma_f32_16x16x32_bf16(xf, wf, c, 0, 0, 0);
    };
    auto compute = [&](const K1Frag& cur, K1Frag& nxt, unsigned so, bool pf) {
        constexpr int PN = NH ? 2 : 0;
        bf16x8 wp[2];
#pragma unroll
        for (int m = 0; m < 18; ++m) {
            const int kk = m / 9, t = m % 9;
            if (t == 0) mm(cur.x[kk][NH], cur.x[kk][NH], asq);
            else if (t >= 7) mm(wp[kk], cur.x[kk][t - 7], acc[t - 7][PN]);
            else if (NH == 0) mm(cur.w[kk][(t + 1) >> 1], cur.x[kk][(t + 1) & 1], acc[(t + 1) & 1][(t + 1) >> 1]);
            else if (t <= 2) mm(cur.w[kk][t + 2], ones, acc[t - 1][3]);
            else mm(cur.w[kk][(t - 3) >> 1], cur.x[kk][(t - 3) & 1], acc[(t - 3) & 1][(t - 3) >> 1]);
            __builtin_amdgcn_sched_barrier(0);
            if (pf && m < 2 * (NW + 1)) rd(nxt, so, m);
            if (m >= 12 - 12 * (1 - kk) + 2 && m < 12 - 12 * (1 - kk) + 3) {
                u32x4 v = __builtin_bit_cast(u32x4, cur.w[kk][PN]);
#pragma unroll
                for (int e = 0; e < 4; ++e) v[e] = patch ? 0x3f803f80u : v[e];
                wp[kk] = __builtin_bit_cast(bf16x8, v);
            }
            __builtin_amdgcn_sched_barrier(0);
        }
    };
    K1Frag fa, fb;
    __builtin_amdgcn_s_barrier();
    __builtin_amdgcn_sched_barrier(0);
    st16wt(ws + WS_O + (size_t)blockIdx.x * 9216 + w * 1024 + l * 16, (u32x4)O_EMPTY);
    if (w == 0) st16wt(ws + WS_O + (size_t)blockIdx.x * 9216 + 8192 + l * 16, (u32x4)O_EMPTY);
    else if (w < 5) st16wt(ws + WS_GR + (size_t)blockIdx.x * 4096 + (w - 1) * 1024 + l * 16, (u32x4)0u);
    __builtin_amdgcn_sched_barrier(0);
    STAMP(0, 2);
    STAMPC(0, 14);
#pragma unroll
    for (int i = 0; i < 2 * (NW + 1); ++i) rd(fa, 0, i);
#pragma unroll 1
    for (int it = 0; it < 8; it += 2) {
        asm volatile("s_waitcnt lgkmcnt(0)" ::: "memory");
        __builtin_amdgcn_s_barrier();
        __builtin_amdgcn_sched_barrier(0);
        compute(fa, fb, 32768, true);
        xa[0] ^= 65536; xa[1] ^= 65536;
#pragma unroll
        for (int n = 0; n < NW; ++n) wa[n] ^= 65536;
        asm volatile("s_waitcnt lgkmcnt(0)" ::: "memory");
        __builtin_amdgcn_s_barrier();
        __builtin_amdgcn_sched_barrier(0);
        compute(fb, fa, 0, true);
    }
    compute(fa, fb, 0, false);
    STAMPC(0, 15);
    STAMP(0, 3);
    STAMP(0, 4);

    float* cs = (float*)(smem + K1_CS);
    float* sx = (float*)(smem + K1_ST);
    if (NH == 1) {
        if (SWAP) {
            if (i16 == 0) { *(f32x4*)(cs + 32 * mw + 4 * g) = acc[0][3]; *(f32x4*)(cs + 32 * mw + 16 + 4 * g) = acc[1][3]; }
        } else {
            if (g == 0) { cs[32 * mw + i16] = acc[0][3][0]; cs[32 * mw + 16 + i16] = acc[1][3][0]; }
        }
    }
    if (odd != NH) {
        constexpr int PN = NH ? 2 : 0;
        if (SWAP) {
            if (g == (NH ? 3 : 0)) { sx[32 * mw + i16] = acc[0][PN][0]; sx[32 * mw + 16 + i16] = acc[1][PN][0]; }
        } else {
            if (i16 == (NH ? 12 : 0)) { *(f32x4*)(sx + 32 * mw + 4 * g) = acc[0][PN]; *(f32x4*)(sx + 32 * mw + 16 + 4 * g) = acc[1][PN]; }
        }
    }
    if (g == q) sx[128 + 32 * mw + 16 * NH + i16] = p == 0 ? asq[0] : p == 1 ? asq[1] : p == 2 ? asq[2] : asq[3];
    __syncthreads();
    STAMP(0, 5);
    if (SWAP) {
        float mean[2], rstd[2];
        f32x4 c4[NN];
#pragma unroll
        for (int mi = 0; mi < 2; ++mi) { mean[mi] = sx[mw * 32 + mi * 16 + i16]; rstd[mi] = sx[128 + mw * 32 + mi * 16 + i16]; }
#pragma unroll
        for (int n = 0; n < NN; ++n) c4[n] = *(const f32x4*)(cs + 16 * (4 * NH + n) + 4 * g);
#pragma unroll
        for (int mi = 0; mi < 2; ++mi) {
            mean[mi] *= (1.0f / 576.0f);
            rstd[mi] = rsqrtf(fmaxf(rstd[mi] * (1.0f / 576.0f) - mean[mi] * mean[mi], 0.f) + LN_EPS);
        }
#pragma unroll
        for (int n = 0; n < NN; ++n)
#pragma unroll
            for (int pr = 0; pr < 2; ++pr)
#pragma unroll
                for (int mi = 0; mi < 2; ++mi) {
                    const float rs = MODE == 0 ? rstd[mi] * es[n][pr] : rstd[mi];
                    const unsigned pk = pk2((acc[mi][n][2 * pr] - mean[mi] * c4[n][2 * pr]) * rs,
                                            (acc[mi][n][2 * pr + 1] - mean[mi] * c4[n][2 * pr + 1]) * rs);
                    *(lds_u32)(ea[n][pr] + mi * 256) = pk;
                    if (MODE == 1) {
                        *(lds_u16)(ev[n][pr] + mi * 608) = (unsigned short)(pk & 0xffff);
                        *(lds_u16)(ev[n][pr] + mi * 608 + 16) = (unsigned short)(pk >> 16);
                    }
                }
    } else {
        f32x4 mean[2], rstd[2];
        float c1[NN];
#pragma unroll
        for (int mi = 0; mi < 2; ++mi) {
            mean[mi] = *(const f32x4*)(sx + mw * 32 + mi * 16 + 4 * g);
            rstd[mi] = *(const f32x4*)(sx + 128 + mw * 32 + mi * 16 + 4 * g);
        }
#pragma unroll
        for (int n = 0; n < NN; ++n) c1[n] = cs[16 * (4 * NH + n) + i16];
#pragma unroll
        for (int mi = 0; mi < 2; ++mi)
#pragma unroll
            for (int e = 0; e < 4; ++e) {
                mean[mi][e] *= (1.0f / 576.0f);
                rstd[mi][e] = rsqrtf(fmaxf(rstd[mi][e] * (1.0f / 576.0f) - mean[mi][e] * mean[mi][e], 0.f) + LN_EPS);
            }
#pragma unroll
        for (int n = 0; n < NN; ++n)
#pragma unroll
            for (int mi = 0; mi < 2; ++mi) {
                u32x2 o;
                o[0] = pk2((acc[mi][n][0] - mean[mi][0] * c1[n]) * rstd[mi][0], (acc[mi][n][1] - mean[mi][1] * c1[n]) * rstd[mi][1]);
                o[1] = pk2((acc[mi][n][2] - mean[mi][2] * c1[n]) * rstd[mi][2], (acc[mi][n][3] - mean[mi][3] * c1[n]) * rstd[mi][3]);
                *(lds_u32x2)(ea[n][0] + mi * 608) = o;
            }
    }
    __syncthreads();
    STAMP(0, 6);
    u32x4 cv[4];
#pragma unroll
    for (int t = 0; t < 4; ++t) cv[t] = *(const u32x4*)(smem + K1_EPI + (tid + 512 * t) * 16);
#pragma unroll
    for (int t = 0; t < 4; ++t)
        if (go[t] >= 0) st16wt(ws + go[t], cv[t]);
    STAMP(0, 7);
}

template <bool ISW>
DI void k1_loader(char* smem, const char* __restrict__ src, const char* __restrict__ wo, char* __restrict__ ws, int mt, int j, int l) {
    unsigned vo[2];
    __builtin_amdgcn_s_setprio(3);
    if (ISW) {
        const int g = l >> 4, i16 = l & 15;
#pragma unroll
        for (int t = 0; t < 2; ++t) {
            int u = (i16 >> 1) ^ (g | (t << 2)); u = u > 6 ? 6 : u;
            vo[t] = g * 3456 + u * 32 + (i16 & 1) * 16;
        }
        src += (216 * j) & ~15;
    } else {
        const int r8 = l >> 3, sl = l & 7;
#pragma unroll
        for (int t = 0; t < 2; ++t) vo[t] = r8 * 1152 + ((sl ^ ((r8 >> 1) | (t << 2))) << 4);
        src += (size_t)mt * 128 * 1152;
    }
    int kc = (2 * mt + j) % 9;
    auto issue = [&](int it) {
        char* st = smem + (it & 3) * 32768 + (ISW ? 16384 : 0);
        const char* sp = src + (ISW ? kc * 221184 : kc * 128);
#pragma unroll
        for (int i = 0; i < 16; ++i)
            glds16(sp + i * (ISW ? 13824 : 9216) + vo[ISW ? (i >> 1) & 1 : i & 1], st + i * 1024);
        kc = kc == 8 ? 0 : kc + 1;
    };
#pragma unroll 1
    for (int it = 0; it < 3; ++it) issue(it);
    asm volatile("s_waitcnt vmcnt(32)" ::: "memory");
    __builtin_amdgcn_s_barrier();
    issue(3);
    const int f0 = blockIdx.x * 2 + (ISW ? 1 : 0);
    const char* wp[2]; int st1[2], st2[2];
#pragma unroll
    for (int t = 0; t < 2; ++t) {
        const int fr = (t == 1 && f0 < 136) ? f0 + 512 : f0;
        const int nt = fr / 18, ks = fr - nt * 18, g = l >> 4;
        const int row0 = ks < 16 ? (2 * ks + (g >> 1)) * 18 + (g & 1) * 8 : ((ks - 16) * 16 + g * 4) * 18 + 16;
        wp[t] = wo + row0 * 1152 + (nt * 16 + (l & 15)) * 2;
        st1[t] = 1152; st2[t] = ks < 16 ? 1152 : 17 * 1152;
    }
    asm volatile("s_waitcnt vmcnt(32)" ::: "memory");
    __builtin_amdgcn_s_barrier();
    issue(4);
    unsigned wv[2][8];
#pragma unroll
    for (int t = 0; t < 2; ++t)
#pragma unroll
        for (int e = 0; e < 8; ++e) {
            if (t == 1 && e == 7) asm volatile("s_waitcnt vmcnt(62)" ::: "memory");
            asm volatile("global_load_ushort %0, %1, off" : "=v"(wv[t][e]) : "v"(wp[t]) : "memory");
            wp[t] += (e & 1) ? st2[t] : st1[t];
        }
#pragma unroll 1
    for (int it = 1; it < 4; ++it) {
        asm volatile("s_waitcnt vmcnt(47)" ::: "memory");
        __builtin_amdgcn_s_barrier();
        issue(it + 4);
    }
    asm volatile("s_waitcnt vmcnt(32)" ::: "memory");
    __builtin_amdgcn_s_barrier();
    issue(8);
    asm volatile("s_waitcnt vmcnt(32)" ::: "memory");
    __builtin_amdgcn_s_barrier();
    asm volatile("s_waitcnt vmcnt(16)" ::: "memory");
    __builtin_amdgcn_s_barrier();
    asm volatile("s_waitcnt vmcnt(0)" ::: "memory");
    __builtin_amdgcn_s_barrier();
    __builtin_amdgcn_sched_barrier(0);
#pragma unroll
    for (int t = 0; t < 2; ++t) {
        u32x4 v;
#pragma unroll
        for (int e = 0; e < 4; ++e) {
            asm volatile("" : "+v"(wv[t][2 * e]), "+v"(wv[t][2 * e + 1]));
            v[e] = wv[t][2 * e] | (wv[t][2 * e + 1] << 16);
        }
        if (t == 0 || f0 < 136) st16wt(ws + WS_WO + (size_t)(f0 + 512 * t) * 1024 + l * 16, v);
    }
    __builtin_amdgcn_s_barrier();
    __builtin_amdgcn_s_barrier();
}

__global__ __launch_bounds__(640) void k1_qkv(const char* __restrict__ x, const char* __restrict__ wqkv,
                                              const char* __restrict__ wo, char* __restrict__ ws) {
    __shared__ __attribute__((aligned(16))) char smem[K1_LDS];
    const int b = blockIdx.x, xcd = b & 7, i = b >> 3;
    const int mt = (xcd >> 1) * 4 + (i & 3), j = (xcd & 1) * 8 + (i >> 2);
    const int w = __builtin_amdgcn_readfirstlane(threadIdx.x >> 6), l = threadIdx.x & 63;
    if (w >= 8) {
        if (w == 8) k1_loader<false>(smem, x, wo, ws, mt, j, l);
        else k1_loader<true>(smem, wqkv, wo, ws, mt, j, l);
    } else if (w < 4) {
        if (j < 10) k1_body<0, 0>(smem, ws, mt, j, w, l);
        else if (j == 10) k1_body<1, 0>(smem, ws, mt, j, w, l);
        else k1_body<2, 0>(smem, ws, mt, j, w, l);
    } else {
        if (j < 10) k1_body<0, 1>(smem, ws, mt, j, w, l);
        else if (j == 10) k1_body<1, 1>(smem, ws, mt, j, w, l);
        else k1_body<2, 1>(smem, ws, mt, j, w, l);
    }
}

#define SB0 __builtin_amdgcn_sched_barrier(0)
#define EX(x) __builtin_amdgcn_exp2f(x)
#define MF(A, B, C) __builtin_amdgcn_mfma_f32_32x32x16_bf16(A, B, C, 0, 0, 0)
#define BC(x) __builtin_bit_cast(bf16x8, x)

DI f32x16 lo16(const f32x32& v) { return __builtin_shufflevector(v, v, 0, 1, 2, 3, 4, 5, 6, 7, 8, 9, 10, 11, 12, 13, 14, 15); }
DI f32x16 hi16(const f32x32& v) { return __builtin_shufflevector(v, v, 16, 17, 18, 19, 20, 21, 22, 23, 24, 25, 26, 27, 28, 29, 30, 31); }
DI bf16x8 ldsb8(const char* p) { return *(const bf16x8*)p; }

DI unsigned pkp(float a, float b) {
    unsigned r = __builtin_bit_cast(unsigned, b);
    asm("v_mov_b32_sdwa %0, %1 dst_sel:WORD_0 dst_unused:UNUSED_PRESERVE src0_sel:WORD_1" : "+v"(r) : "v"(a));
    return r;
}

struct AttnCarry { float e0, e1, e2, e3; u32x4 pc; bf16x8 v0, v1, v2, vl; };

template <int O> DI void ldv3(AttnCarry& cy, unsigned a, unsigned long long m) {
    asm volatile("s_mov_b64 exec, %4\n\tds_read_b128 %0, %3 offset:%5\n\tds_read_b128 %1, %3 offset:%6\n\t"
                 "ds_read_b128 %2, %3 offset:%7\n\ts_mov_b64 exec, -1"
                 : "+v"(cy.v0), "+v"(cy.v1), "+v"(cy.v2) : "v"(a), "s"(m), "n"(O), "n"(O + 608), "n"(O + 4864));
}
template <int O> DI void ldv1(AttnCarry& cy, unsigned a, unsigned long long m) {
    asm volatile("s_mov_b64 exec, %2\n\tds_read_b128 %0, %1 offset:%3\n\ts_mov_b64 exec, -1"
                 : "+v"(cy.vl) : "v"(a), "s"(m), "n"(O + 5472));
}

template <int PP>
DI void attn_step(const char* knext, unsigned vcur, const f32x16& sa, const f32x16& sb, f32x16& na, f32x16& nb,
                  f32x16& o, AttnCarry& cy, const bf16x8& q1, const s16x4& q2a, const s16x4& q2b, const f32x32& cinit,
                  const u32x4& k2n, unsigned long long vrow) {
    constexpr int VO = ((PP + 3) & 3) * 1216;
    s16x4 k2 = __builtin_bit_cast(s16x4, (PP & 2) ? __builtin_shufflevector(k2n, k2n, 2, 3) : __builtin_shufflevector(k2n, k2n, 0, 1));
    u32x4 pa, pb;
    float a0, a1, a2, a3, b0, b1, b2, b3;
#define EXA(s, i) a0 = EX(s[i]); a1 = EX(s[i + 1]); a2 = EX(s[i + 2]); a3 = EX(s[i + 3])
#define EXB(s, i) b0 = EX(s[i]); b1 = EX(s[i + 1]); b2 = EX(s[i + 2]); b3 = EX(s[i + 3])
    bf16x8 ka = ldsb8(knext), kb = ldsb8(knext + 4096);
    SB0;
    ldv3<VO>(cy, vcur, vrow);
    SB0;
    f32x32 n2 = __builtin_amdgcn_mfma_f32_32x32x4bf16_1k(k2, (PP & 1) ? q2b : q2a, cinit, 0, 0, 0);
    SB0;
    cy.pc[2] = pkp(cy.e0, cy.e1); cy.pc[3] = pkp(cy.e2, cy.e3);
    SB0;
    EXA(sa, 0);
    SB0;
    o = MF(cy.vl, BC(cy.pc), o);
    SB0;
    EXB(sa, 4);
    SB0;
    ldv1<VO>(cy, vcur, vrow);
    SB0;
    pa[0] = pkp(a0, a1); pa[1] = pkp(a2, a3);
    SB0;
    EXA(sa, 8);
    SB0;
    pa[2] = pkp(b0, b1); pa[3] = pkp(b2, b3);
    SB0;
    asm volatile("s_waitcnt lgkmcnt(1)");
    SB0;
    na = MF(ka, q1, lo16(n2));
    SB0;
    EXB(sa, 12);
    SB0;
    pb[0] = pkp(a0, a1); pb[1] = pkp(a2, a3);
    SB0;
    o = MF(cy.v0, BC(pa), o);
    SB0;
    EXA(sb, 0);
    SB0;
    pb[2] = pkp(b0, b1); pb[3] = pkp(b2, b3);
    SB0;
    asm volatile("s_waitcnt lgkmcnt(0)");
    SB0;
    nb = MF(kb, q1, hi16(n2));
    SB0;
    EXB(sb, 4);
    SB0;
    pa[0] = pkp(a0, a1); pa[1] = pkp(a2, a3);
    SB0;
    o = MF(cy.v1, BC(pb), o);
    SB0;
    EXA(sb, 8);
    SB0;
    pa[2] = pkp(b0, b1); pa[3] = pkp(b2, b3);
    SB0;
    cy.e0 = EX(sb[12]); cy.e1 = EX(sb[13]);
    SB0;
    o = MF(cy.v2, BC(pa), o);
    SB0;
    cy.e2 = EX(sb[14]); cy.e3 = EX(sb[15]);
    SB0;
    cy.pc[0] = pkp(a0, a1); cy.pc[1] = pkp(a2, a3);
    SB0;
#undef EXA
#undef EXB
}

template <bool FIRST>
DI f32x16 attn_pass(const char* smem, int l, const bf16x8& q1, const s16x4& q2a, const s16x4& q2b, const f32x32& cinit) {
    const int r = l & 31, h = l >> 5;
    const char* kp = smem + h * 512 + r * 16;
    unsigned vp = (unsigned)(uintptr_t)smem + 9216 + h * 304 + r * 16;
    const unsigned long long vrow = 0x0007ffff0007ffffull;
    f32x16 o = (f32x16)0.f;
    f32x16 sa, sb, ta, tb;
    AttnCarry cy;
    cy.e0 = cy.e1 = cy.e2 = cy.e3 = 0.f; cy.pc = (u32x4)0u;
    cy.v0 = cy.v1 = cy.v2 = cy.vl = BC((u32x4)0u);
    u32x4 k2 = *(const u32x4*)(kp + 8192);
    {
        s16x4 k2p = __builtin_bit_cast(s16x4, __builtin_shufflevector(k2, k2, 0, 1));
        f32x32 n2 = __builtin_amdgcn_mfma_f32_32x32x4bf16_1k(k2p, q2a, cinit, 0, 0, 0);
        sa = MF(ldsb8(kp), q1, lo16(n2));
        sb = MF(ldsb8(kp + 4096), q1, hi16(n2));
    }
#pragma unroll 1
    for (int c = 0; c < 8; ++c) {
        if (c == 4) __builtin_amdgcn_s_setprio(2);
        else if (c == 6) __builtin_amdgcn_s_setprio(1);
        else if (c == 7) __builtin_amdgcn_s_setprio(0);
        attn_step<1>(kp + 1024, vp, sa, sb, ta, tb, o, cy, q1, q2a, q2b, cinit, k2, vrow);
        attn_step<2>(kp + 2048, vp, ta, tb, sa, sb, o, cy, q1, q2a, q2b, cinit, k2, vrow);
        attn_step<3>(kp + 3072, vp, sa, sb, ta, tb, o, cy, q1, q2a, q2b, cinit, k2, vrow);
        if (FIRST && c == 0) {
            asm volatile("s_waitcnt vmcnt(0)" ::: "memory");
            __builtin_amdgcn_s_barrier();
        }
        k2 = *(const u32x4*)(kp + KCH + 8192);
        attn_step<0>(kp + KCH, vp, ta, tb, sa, sb, o, cy, q1, q2a, q2b, cinit, k2, vrow);
        kp += KCH; vp += KCH;
    }
    cy.pc[2] = pkp(cy.e0, cy.e1); cy.pc[3] = pkp(cy.e2, cy.e3);
    SB0;
    asm volatile("s_nop 1");
    SB0;
    o = MF(cy.vl, BC(cy.pc), o);
    return o;
}

DI void attn_wave(char* smem, char* __restrict__ ws, int head, int qb, int w, int l) {
    const int r = l & 31, h = l >> 5;
    const int qt = qb * 8 + w;
    STAMP(1, 0);
    __builtin_amdgcn_s_setprio(3);
    const char* qp = ws + WS_Q + (size_t)head * QIMG + (size_t)qb * QCH;
    bf16x8 q1; unsigned q2;
    asm volatile("global_load_dwordx4 %0, %1, off" : "=v"(q1) : "v"(qp + w * 1024 + h * 512 + r * 16) : "memory");
    asm volatile("global_load_dword %0, %1, off" : "=v"(q2) : "v"(qp + 8192 + (w >> 2) * 512 + r * 16 + (w & 3) * 4) : "memory");
    SB0;
    {
        const char* src = ws + WS_KV + (size_t)head * KVIMG + w * 1024 + l * 16;
        char* dst = smem + w * 1024;
#pragma unroll
        for (int t = 0; t < 19; ++t) glds16(src + t * 8192, dst + t * 8192);
    }
    asm volatile("s_waitcnt vmcnt(16)" ::: "memory");
    SB0;
    asm volatile("" : "+v"(q1), "+v"(q2));
    u32x2 qa; qa[0] = q2; qa[1] = 0u;
    u32x2 qbv; qbv[0] = 0u; qbv[1] = q2;
    const s16x4 q2a = __builtin_bit_cast(s16x4, qa), q2b = __builtin_bit_cast(s16x4, qbv);
    f32x32 cinit = (f32x32)0.f;
    __builtin_amdgcn_s_barrier();
    SB0;
    STAMP(1, 2);
    STAMPC(1, 14);
    f32x16 o = attn_pass<true>(smem, l, q1, q2a, q2b, cinit);
    STAMP(1, 3);
    STAMPC(1, 15);
    auto denom = [&]() {
        const float o10 = o[10];
        const unsigned lb = __builtin_bit_cast(unsigned, o10);
        auto sw = __builtin_amdgcn_permlane32_swap(lb, lb, false, false);
        const unsigned l0 = sw[0];
        return __builtin_bit_cast(float, l0);
    };
    float lsum = denom();
    bool ok = (lsum >= 9.0e-13f) && (lsum <= 1.0e18f);
#pragma unroll
    for (int e = 0; e < 8; ++e) ok = ok && (fabsf(o[e]) <= 3.0e38f);
    ok = ok && (h == 1 || ((fabsf(o[8]) <= 3.0e38f) && (fabsf(o[9]) <= 3.0e38f)));
    if (!__all(ok)) {
        const char* kp = smem + h * 512 + r * 16;
        float m = -3.0e38f;
#pragma unroll 1
        for (int c = 0; c < 8; ++c) {
            u32x4 k2 = *(const u32x4*)(kp + 8192);
#pragma unroll
            for (int pp = 0; pp < 4; ++pp) {
                s16x4 k2p = __builtin_bit_cast(s16x4, (pp & 2) ? __builtin_shufflevector(k2, k2, 2, 3) : __builtin_shufflevector(k2, k2, 0, 1));
                f32x32 n2 = __builtin_amdgcn_mfma_f32_32x32x4bf16_1k(k2p, (pp & 1) ? q2b : q2a, cinit, 0, 0, 0);
                f32x16 sa = MF(ldsb8(kp + pp * 1024), q1, lo16(n2));
                f32x16 sb = MF(ldsb8(kp + pp * 1024 + 4096), q1, hi16(n2));
#pragma unroll
                for (int e = 0; e < 16; ++e) m = fmaxf(m, fmaxf(sa[e], sb[e]));
            }
            kp += KCH;
        }
        m = fmaxf(m, __shfl_xor(m, 32));
        cinit = (f32x32)(-m);
        o = attn_pass<false>(smem, l, q1, q2a, q2b, cinit);
        lsum = denom();
    }
    const float inv = 1.0f / lsum;
    char* op = ws + WS_O + ((size_t)(qt * 2 + (r >> 4)) * 18) * 1024;
    u32x2 a, c;
    auto pko = [&](float u, float v) { return min(pk2(u * inv, v * inv), O_EMPTY - 1u); };
    a[0] = pko(o[0], o[1]); a[1] = pko(o[2], o[3]);
    c[0] = pko(o[4], o[5]); c[1] = pko(o[6], o[7]);
    char* mp = op + (head >> 1) * 1024 + (head & 1) * 512 + (r & 15) * 16 + h * 8;
    st8wt(mp, a);
    st8wt(mp + 256, c);
    if (h == 0) st4wt(ws + WS_O + ((size_t)(qt * 2 + (head >> 4)) * 18 + 16 + ((head >> 3) & 1)) * 1024 + (head & 7) * 128 + r * 4, pko(o[8], o[9]));
    if (l == 0) asm volatile("ds_add_u32 %0, %1" :: "v"(cnt_addr(smem)), "v"(1u) : "memory");
    STAMP(1, 4);
}

__global__ __launch_bounds__(576) void k2_attn(const char* __restrict__ x, const char* __restrict__ wr, const char* __restrict__ wg,
                                               const char* __restrict__ wu, const char* __restrict__ wd, char* __restrict__ ws,
                                               char* __restrict__ out) {
    __shared__ __attribute__((aligned(16))) char smem[163840];
    const int b = blockIdx.x, xcd = b & 7, i = b >> 3;
    const int tid = threadIdx.x;
    const int w = __builtin_amdgcn_readfirstlane(tid >> 6), l = tid & 63;
    const int mb = xcd * 8 + (i >> 2), ns = i & 3;
    if (w == 8) {
        asm volatile("ds_write_b32 %0, %1\n\ts_waitcnt lgkmcnt(0)" :: "v"(cnt_addr(smem)), "v"(0u) : "memory");
        __builtin_amdgcn_s_barrier();
        __builtin_amdgcn_s_barrier();
        char* sink = smem + KVIMG;
#pragma unroll 1
        for (int t = 0; t < 21; ++t) {
            int P = t * 32 + i; P = P < 648 ? P : 647;
            const int u = P * 64 + l;
            const int row = ((u >> 3) * 7282) >> 16;
            const unsigned o = row * 9216 + xcd * 1152 + (u - row * 72) * 16;
            glds16(wg + o, sink);
            glds16(wu + o, sink + 1024);
            glds16(wd + xcd * 663552 + u * 16, sink + 2048);
            glds16(ws + WS_WO + u * 16, sink + 3072);
            asm volatile("s_waitcnt vmcnt(48)" ::: "memory");
        }
#pragma unroll 1
        for (int t = 0; t < 9; ++t) glds16(x + xcd * 294912 + (t * 32 + i) * 1024 + l * 16, sink + 4096);
        asm volatile("s_waitcnt vmcnt(0)" ::: "memory");
    } else {
        attn_wave(smem, ws, xcd * 4 + (i >> 3), i & 7, w, l);
    }
    SB0;
    const int g = l >> 4, i16 = l & 15;
    const int nt = ns * 9 + w;
    const int c0 = nt * 16 + 4 * g;
    const char* wsrc = ws + WS_WO + (size_t)nt * 18432 + l * 16;
    bf16x8 wf[18];
#pragma unroll
    for (int ks = 0; ks < 18; ++ks) {
        wf[ks] = *(const bf16x8*)(wsrc + ks * 1024);
        if (ks % WBATCH == WBATCH - 1 && ks < 17) {
            SB0;
            if (cnt_peek(smem) < 8u) asm volatile("s_waitcnt vmcnt(0)" ::: "memory");
            SB0;
        }
    }
    u32x2 xr[2];
#pragma unroll
    for (int mi = 0; mi < 2; ++mi) xr[mi] = *(const u32x2*)(x + (size_t)(mb * 32 + mi * 16 + i16) * 1152 + c0 * 2);
    u32x2 wrv = *(const u32x2*)(wr + c0 * 2);
    float wsum = bf2f(((const unsigned short*)wr)[tid]);
    SB0;
    if (w == 8) {
        for (unsigned spins = 0; spins < SPIN_CAP; ++spins) {
            if (cnt_peek(smem) != 0u) break;
            __builtin_amdgcn_s_sleep(8);
        }
    }
    STAMP(2, 0);
    u32x4 ot[4];
    {
        const char* osrc = ws + WS_O + (size_t)mb * 36864 + w * 1024 + l * 16;
#pragma unroll 1
        for (unsigned spins = 0; spins < SPIN_CAP; ++spins) {
            asm volatile("global_load_dwordx4 %0, %4, off sc1\n\tglobal_load_dwordx4 %1, %5, off sc1\n\t"
                         "global_load_dwordx4 %2, %6, off sc1\n\tglobal_load_dwordx4 %3, %7, off sc1\n\ts_waitcnt vmcnt(0)"
                         : "=&v"(ot[0]), "=&v"(ot[1]), "=&v"(ot[2]), "=&v"(ot[3])
                         : "v"(osrc), "v"(osrc + 9216), "v"(osrc + 18432), "v"(osrc + 27648) : "memory");
            unsigned m = 0u;
#pragma unroll
            for (int t = 0; t < 4; ++t) m = max(m, max(max(ot[t][0], ot[t][1]), max(ot[t][2], ot[t][3])));
            if (__all(m != O_EMPTY)) break;
            __builtin_amdgcn_s_sleep(OSLEEP);
        }
    }
    STAMP(2, 7);
#pragma unroll
    for (int ks = 0; ks < 18; ++ks) asm volatile("" : "+v"(wf[ks]));
    asm volatile("" : "+v"(xr[0]), "+v"(xr[1]), "+v"(wrv), "+v"(wsum));
    SB0;
    SB0;
    asm volatile("" ::: "memory");
    __builtin_amdgcn_s_barrier();
    asm volatile("" ::: "memory");
    STAMP(2, 6);
#pragma unroll
    for (int t = 0; t < 4; ++t) {
        if (w >= 7 && (t & 1)) {
            const int head = ((t >> 1) * 2 + (w - 7)) * 8 + (l >> 3);
            char* d = smem + (((l >> 2) & 1) * 18 + 16 + (head >> 4)) * 1024 + ((head >> 2) & 3) * 256 + (l & 3) * 64 + (head & 3) * 4;
#pragma unroll
            for (int k = 0; k < 4; ++k) *(unsigned*)(d + k * 16) = ot[t][k];
        } else {
            *(u32x4*)(smem + w * 1024 + l * 16 + t * 9216) = ot[t];
        }
    }
    const int fb = b < 144 ? b : b - 112;
    const int f = (fb & 7) * 18 + (fb >> 3);
    const int kr = tid >> 2, cq = tid & 3, rg = l >> 3;
    const unsigned go = kr * (F_ * 2) + f * 64 + cq * 16;
    u32x4 gv[4], uv[4], dv[4];
    __syncthreads();
    STAMP(2, 1);
    f32x4 acc[2] = {(f32x4)0.f, (f32x4)0.f};
    bf16x8 of[3][4];
    auto rdo = [&](int gq) {
#pragma unroll
        for (int q = 0; q < 4; ++q) of[gq % 3][q] = *(const bf16x8*)(smem + ((q & 1) * 18 + gq * 2 + (q >> 1)) * 1024 + l * 16);
    };
    rdo(0); rdo(1);
#pragma unroll
    for (int gq = 0; gq < 9; ++gq) {
        SB0;
        if (gq < 7) rdo(gq + 2);
        if (gq & 1) gv[gq >> 1] = *(const u32x4*)(wg + go + (gq >> 1) * 144 * (F_ * 2));
        SB0;
#pragma unroll
        for (int q = 0; q < 4; ++q)
            acc[q & 1] = __builtin_amdgcn_mfma_f32_16x16x32_bf16(wf[gq * 2 + (q >> 1)], of[gq % 3][q], acc[q & 1], 0, 0, 0);
    }
    SB0;
#pragma unroll
    for (int t = 0; t < 4; ++t) uv[t] = *(const u32x4*)(wu + go + t * 144 * (F_ * 2));
    SB0;
    STAMP(2, 2);
    char* ht = smem + 36864;
    float* red = (float*)(smem + 36864 + 9728);
    float* tot = red + 9 * 96;
    float* wsm = tot + 96;
    float* sm = wsm + 16;
    {
        const float w0 = bflo(wrv[0]), w1 = bfhi(wrv[0]), w2 = bflo(wrv[1]), w3 = bfhi(wrv[1]);
        float s[2], qq[2], e[2];
#pragma unroll
        for (int mi = 0; mi < 2; ++mi) {
            u32x2 hv;
            hv[0] = pk2(bflo(xr[mi][0]) + rbf(acc[mi][0]), bfhi(xr[mi][0]) + rbf(acc[mi][1]));
            hv[1] = pk2(bflo(xr[mi][1]) + rbf(acc[mi][2]), bfhi(xr[mi][1]) + rbf(acc[mi][3]));
            *(u32x2*)(ht + (mi * 16 + i16) * 304 + w * 32 + g * 8) = hv;
            const float h0 = bflo(hv[0]), h1 = bfhi(hv[0]), h2 = bflo(hv[1]), h3 = bfhi(hv[1]);
            s[mi] = (h0 + h1) + (h2 + h3);
            qq[mi] = (h0 * h0 + h1 * h1) + (h2 * h2 + h3 * h3);
            e[mi] = (h0 * w0 + h1 * w1) + (h2 * w2 + h3 * w3);
        }
        const float s1 = sw16(s[0], s[1]), q1 = sw16(qq[0], qq[1]), e1 = sw16(e[0], e[1]);
        red[w * 96 + l] = sw32(s1, q1);
        const float e2 = sw32(e1, e1);
        if (l < 32) red[w * 96 + 64 + l] = e2;
        wsum += rorf<1>(wsum); wsum += rorf<2>(wsum); wsum += rorf<4>(wsum); wsum += rorf<8>(wsum);
        wsum = sw16(wsum, wsum); wsum = sw32(wsum, wsum);
        if (l == 0) wsm[w] = wsum;
    }
    __syncthreads();
    STAMP(2, 3);
    char* gr = ws + WS_GR3 + (size_t)mb * 3072 + tid * 8;
    if (tid < 96) {
        float a = 0.f;
#pragma unroll
        for (int ww = 0; ww < 9; ++ww) a += red[ww * 96 + tid];
        put_granule(gr + ns * 768, a);
    }
    SB0;
#pragma unroll
    for (int t = 0; t < 4; ++t) dv[t] = *(const u32x4*)(wd + (size_t)(f * 32 + rg + 8 * t) * 1152 + w * 128 + (l & 7) * 16);
    SB0;
    f32x2 gf[4][4], uf[4][4];
#pragma unroll
    for (int t = 0; t < 4; ++t)
#pragma unroll
        for (int e = 0; e < 4; ++e) {
            gf[t][e][0] = bflo(gv[t][e]); gf[t][e][1] = bfhi(gv[t][e]);
            uf[t][e][0] = bflo(uv[t][e]); uf[t][e][1] = bfhi(uv[t][e]);
            asm volatile("" : "+v"(gf[t][e]), "+v"(uf[t][e]));
        }
    SB0;
    if (tid < 96) {
        float v[4];
        get_granules<4>(gr, 768, v);
        tot[tid] = (v[0] + v[1]) + (v[2] + v[3]);
    }
    const int row = tid / 18, ch = tid - row * 18;
    const u32x4 hv = *(const u32x4*)(ht + row * 304 + ch * 16);
    char* optr = out + (size_t)(mb * 32 + row) * 1152 + ns * 288 + ch * 16;
    float wt = 0.f;
#pragma unroll
    for (int ww = 0; ww < 9; ++ww) wt += wsm[ww];
    __syncthreads();
    STAMP(2, 4);
    int br;
    {
        const int r = l & 31;
        const float mean = tot[r] * (1.0f / 576.0f);
        const float rstd = rsqrtf(fmaxf(tot[32 + r] * (1.0f / 576.0f) - mean * mean, 0.f) + LN_EPS);
        const float rv = (tot[64 + r] - mean * wt) * rstd;
        br = argmax_lane(rv);
        if (ns == 0 && w == 0) {
            const unsigned rb = __builtin_amdgcn_readlane(__builtin_bit_cast(int, rv), br);
            if (l < 16) put_granule_u(ws + WS_SCORE + l * 2304 + mb * 8, rb);
            if (l == br) {
                char* cg = ws + WS_CANDG + mb * 8;
                put_granule_u(cg + 512, mb * 32 + r); put_granule(cg + 1024, mean); put_granule(cg + 1536, rstd);
            }
        }
    }
    if (row == br) {
#pragma unroll
        for (int k = 0; k < 4; ++k) put_granule_u(ws + WS_ROWG + ((size_t)mb * 288 + ns * 72 + ch * 4 + k) * 8, hv[k]);
    }
    u32x4 ov;
    {
        const float mean = tot[row] * (1.0f / 576.0f);
        const float rstd = rsqrtf(fmaxf(tot[32 + row] * (1.0f / 576.0f) - mean * mean, 0.f) + LN_EPS);
#pragma unroll
        for (int k = 0; k < 4; ++k) {
            const float a = bflo(hv[k]), c = bfhi(hv[k]);
            ov[k] = pk2(a + rbf((a - mean) * rstd), c + rbf((c - mean) * rstd));
        }
        if (row != br) *(u32x4*)optr = ov;
    }
    f32x2 df[4][4];
#pragma unroll
    for (int t = 0; t < 4; ++t)
#pragma unroll
        for (int e = 0; e < 4; ++e) {
            df[t][e][0] = bflo(dv[t][e]); df[t][e][1] = bfhi(dv[t][e]);
            asm volatile("" : "+v"(df[t][e]));
        }
    STAMP(2, 5);
    SB0;
    STAMP(3, 0);
    if (w == 0) {
        float cand[1];
        get_granules<1, CSLEEP>(ws + WS_SCORE + (b & 15) * 2304 + l * 8, 0, cand);
        const int bl0 = argmax_lane(cand[0]);
        if (l == bl0) { wsm[12] = cand[0]; wsm[13] = __builtin_bit_cast(float, bl0); }
    }
    __syncthreads();
    const float best = wsm[12], blf = wsm[13];
    const int bl = __builtin_amdgcn_readfirstlane(__builtin_bit_cast(int, blf));
    if (row == br && bl != mb) *(u32x4*)optr = ov;
    if (b >= 144) return;
    STAMP(3, 1);
    float hr[7], hf[1];
    {
        const char* rp = ws + WS_ROWG + ((size_t)bl * 288 + (kr >> 1)) * 8;
        const char* cp = ws + WS_CANDG + bl * 8;
        for (unsigned spins = 0; spins < SPIN_CAP; ++spins) {
            bool ok = true;
#pragma unroll
            for (int k = 0; k < 7; ++k) {
                const unsigned long long g8 = __hip_atomic_load((gu64*)(k < 4 ? rp + k * 576 : cp + (k - 3) * 512), RLX_AGENT);
                hr[k] = __builtin_bit_cast(float, (unsigned)g8); ok &= (g8 >> 32) == 1ull;
            }
            if (__all(ok)) break;
            __builtin_amdgcn_s_sleep(1);
        }
    }
    const float bidxf = hr[4], bmean = hr[5], brstd = hr[6];
    const int bidx = __builtin_bit_cast(int, bidxf) & (S_ - 1);
    f32x2 ga2[4], ua2[4];
#pragma unroll
    for (int e = 0; e < 4; ++e) { ga2[e] = (f32x2)0.f; ua2[e] = (f32x2)0.f; }
#pragma unroll
    for (int t = 0; t < 4; ++t) {
        const float hrt = hr[t];
        const unsigned hp = __builtin_bit_cast(unsigned, hrt);
        const f32x2 xs = (f32x2)rbf((((kr & 1) ? bfhi(hp) : bflo(hp)) - bmean) * brstd);
#pragma unroll
        for (int e = 0; e < 4; ++e) {
            ga2[e] = __builtin_elementwise_fma(xs, gf[t][e], ga2[e]);
            ua2[e] = __builtin_elementwise_fma(xs, uf[t][e], ua2[e]);
        }
    }
    float ga[8], ua[8];
#pragma unroll
    for (int e = 0; e < 4; ++e) { ga[2 * e] = ga2[e][0]; ga[2 * e + 1] = ga2[e][1]; ua[2 * e] = ua2[e][0]; ua[2 * e + 1] = ua2[e][1]; }
    STAMP(3, 2);
    {
        float v[8]; f32x4 z;
#pragma unroll
        for (int e = 0; e < 8; ++e) v[e] = sw32(ga[e], ua[e]);
#pragma unroll
        for (int e = 0; e < 4; ++e) { float t = sw16(v[e], v[e + 4]); t += rorf<8>(t); t += rorf<4>(t); z[e] = t; }
        if ((l & 12) == 0) *(f32x4*)(sm + w * 64 + (l >> 5) * 32 + cq * 8 + ((l >> 4) & 1) * 4) = z;
    }
    __syncthreads();
    STAMP(3, 3);
    float act;
    {
        float a = 0.f;
#pragma unroll
        for (int p = 0; p < 9; ++p) a += sm[p * 64 + l];
        const unsigned ab = __builtin_bit_cast(unsigned, rbf(a));
        auto r = __builtin_amdgcn_permlane32_swap(ab, ab, false, false);
        const unsigned r0 = r[0], r1 = r[1];
        const float gt = __builtin_bit_cast(float, r0), up = __builtin_bit_cast(float, r1);
        act = rbf(rbf(gt / (1.0f + __expf(-gt))) * up);
    }
    f32x2 y2[4];
#pragma unroll
    for (int e = 0; e < 4; ++e) y2[e] = (f32x2)0.f;
#pragma unroll
    for (int t = 0; t < 4; ++t) {
        const f32x2 a = (f32x2)__shfl(act, rg + 8 * t);
#pragma unroll
        for (int e = 0; e < 4; ++e) y2[e] = __builtin_elementwise_fma(a, df[t][e], y2[e]);
    }
    float ya[8];
#pragma unroll
    for (int e = 0; e < 4; ++e) { ya[2 * e] = y2[e][0]; ya[2 * e + 1] = y2[e][1]; }
    {
        float z[4];
#pragma unroll
        for (int e = 0; e < 4; ++e) z[e] = sw32(ya[e], ya[e + 4]);
        float u0 = sw16(z[0], z[2]), u1 = sw16(z[1], z[3]);
        u0 += rorf<8>(u0); u1 += rorf<8>(u1);
        put_granule(ws + WS_GR4 + ((size_t)f * 576 + w * 64 + (l & 7) * 8 + rg) * 8, (l & 8) ? u1 : u0);
    }
    STAMP(3, 4);
    if (b < 36) {
        get_granules<1>(ws + WS_ROWG + ((size_t)bl * 288 + b * 8 + ((l & 15) >> 1)) * 8, 0, hf);
        float v[4];
        get_granules<4, RSLEEP>(ws + WS_GR4 + ((size_t)(tid >> 4) * 576 + b * 16 + (l & 15)) * 8, 36 * 576 * 8, v);
        float a = (v[0] + v[1]) + (v[2] + v[3]);
        a = sw16(a, a); a = sw32(a, a);
        if (l < 16) sm[576 + w * 16 + l] = a;
        __syncthreads();
        if (tid < 16) {
            float y = 0.f;
#pragma unroll
            for (int t = 0; t < 9; ++t) y += sm[576 + t * 16 + tid];
            const unsigned hp = __builtin_bit_cast(unsigned, hf[0]);
            const float hv1 = (tid & 1) ? bfhi(hp) : bflo(hp);
            const float sg = rbf(1.0f / (1.0f + __expf(-rbf(best))));
            const float hn = rbf((hv1 - bmean) * brstd);
            const float upd = rbf(rbf(y) * sg);
            __bf16 o = (__bf16)(hv1 + rbf(hn + upd));
            ((unsigned short*)out)[(size_t)bidx * 576 + b * 16 + tid] = __builtin_bit_cast(unsigned short, o);
        }
        STAMP(3, 5);
    }
}

extern "C" void kernel_launch(void* const* d_in, const int* in_sizes, int n_in,
                              void* d_out, int out_size, void* d_ws, size_t ws_size,
                              hipStream_t stream) {
    const char* x = (const char*)d_in[0];
    const char* wqkv = (const char*)d_in[1];
    const char* wo = (const char*)d_in[2];
    const char* wr = (const char*)d_in[4];
    const char* wg = (const char*)d_in[5];
    const char* wu = (const char*)d_in[6];
    const char* wd = (const char*)d_in[7];
    char* ws = (char*)d_ws;
    char* out = (char*)d_out;
    k1_qkv<<<256, 640, 0, stream>>>(x, wqkv, wo, ws);
    k2_attn<<<256, 576, 0, stream>>>(x, wr, wg, wu, wd, ws, out);
}
```
